```python
import torch

def _softmax(x, axis=-1):
    return torch.softmax(x, dim=axis)

def _gelu(x):
    return torch.nn.functional.gelu(x, approximate='tanh')

def _silu(x):
    return torch.nn.functional.silu(x)

def _sigmoid(x):
    return torch.sigmoid(x)

def _layernorm(x, axis=-1, eps=1e-05):
    m = x.mean(dim=axis, keepdim=True)
    v = x.var(dim=axis, keepdim=True, unbiased=False)
    return (x - m) / torch.sqrt(v + eps)

def _scatter_add(tgt, idx, src, axis=0):
    return tgt.clone().index_add_(axis, idx, src)

def _segment_sum(src, seg, n):
    out = torch.zeros((n,) + src.shape[1:], dtype=src.dtype)
    return out.index_add_(0, seg, src)

def _pad(x, pw):
    flat = tuple((v for pair in reversed(pw) for v in pair))
    return torch.nn.functional.pad(x, flat)
kernel_name = 'gated_mix__linear_attn_block__memory_aug_recurrent_mem_tr__prompt'

def setup_inputs():

    _w = lambda a: (lambda t: t.to(torch.bfloat16) if t.is_floating_point() else t)(torch.as_tensor(a))

    def _A_setup():
        B, S, D = (1, 2048, 448)
        return {'x': _w((lambda *s: torch.randn(*s, dtype=torch.bfloat16) * 0.05)(B, S, D)), 'wqkv': _w((lambda *s: torch.randn(*s, dtype=torch.bfloat16) * 1.6)(D, 3 * D)), 'wo': _w((lambda *s: torch.randn(*s, dtype=torch.bfloat16) * 0.1)(D, D))}
    _A_in = _A_setup()

    def _B_setup():
        B, S, Dm = (1, 2048, 448)
        H, Dh = (16, 256)
        M, seg = (8, 32)
        seg = min(seg, S)
        S = S // seg * seg
        return {'x': _w((lambda *s: torch.randn(*s, dtype=torch.bfloat16) * 0.05)(B, S, Dm)), 'mem0': _w((lambda *s: torch.randn(*s, dtype=torch.bfloat16) * 0.8)(B, M, Dm)), 'Wq': _w((lambda *s: torch.randn(*s, dtype=torch.bfloat16) * 3.2)(Dm, H, Dh) * Dm ** (-0.5)), 'Wk': _w((lambda *s: torch.randn(*s, dtype=torch.bfloat16) * 3.2)(Dm, H, Dh) * Dm ** (-0.5)), 'Wv': _w((lambda *s: torch.randn(*s, dtype=torch.bfloat16) * 0.8)(Dm, H, Dh) * Dm ** (-0.5)), 'Wo': _w((lambda *s: torch.randn(*s, dtype=torch.bfloat16) * 0.8)(H, Dh, Dm) * (H * Dh) ** (-0.5)), 'seg_len': seg}
    _B_in = _B_setup()
    return {'A_x': _A_in['x'], 'A_wqkv': _A_in['wqkv'], 'A_wo': _A_in['wo'], 'B_x': _B_in['x'], 'B_mem0': _B_in['mem0'], 'B_Wq': _B_in['Wq'], 'B_Wk': _B_in['Wk'], 'B_Wv': _B_in['Wv'], 'B_Wo': _B_in['Wo'], 'B_seg_len': _B_in['seg_len'], 'gate_w': _w((lambda *s: torch.randn(*s, dtype=torch.bfloat16) * 0.05)(448) * 16.0)}

def reference(A_x, A_wqkv, A_wo, B_x, B_mem0, B_Wq, B_Wk, B_Wv, B_Wo, B_seg_len, gate_w):

    def _A_ref(x, wqkv, wo):
        B, S, D = x.shape
        H = 16
        Dh = D // H
        qkv = (lambda x, *d: x.permute(*d))((x @ wqkv).reshape(B, S, 3, H, Dh), 2, 0, 3, 1, 4)
        q, k, v = (qkv[0], qkv[1], qkv[2])
        phi = lambda t: torch.where(t > 0, t + 1, torch.exp(t)) + 1e-06
        pq, pk = (phi(q), phi(k))
        kv = torch.einsum('bhsd,bhse->bhde', pk, v)
        z = torch.einsum('bhsd->bhd', pk)
        num = torch.einsum('bhqd,bhde->bhqe', pq, kv)
        den = torch.einsum('bhqd,bhd->bhq', pq, z)[..., None] + 1e-06
        o = num / den
        return (lambda x, *d: x.permute(*d))(o, 0, 2, 1, 3).reshape(B, S, D) @ wo

    def _B_ref(x, mem0, Wq, Wk, Wv, Wo, seg_len):
        B, S, Dm = x.shape
        H, Dh = (Wq.shape[1], Wq.shape[2])
        M = mem0.shape[1]
        scale = Dh ** (-0.5)
        n_seg = S // seg_len
        L = M + seg_len
        idx = torch.arange(L)
        mask = (idx[:, None] < M) | (idx[:, None] >= idx[None, :])
        mem = mem0
        outs = []
        for s in range(n_seg):
            xs = x[:, s * seg_len:(s + 1) * seg_len, :].clone()
            h = torch.concatenate([mem, xs], axis=1)
            q = torch.einsum('btd,dhe->bhte', h, Wq)
            k = torch.einsum('btd,dhe->bhte', h, Wk)
            v = torch.einsum('btd,dhe->bhte', h, Wv)
            scores = torch.einsum('bhqe,bhke->bhqk', q, k) * scale
            scores = torch.where(mask[None, None], scores, -1000000000.0)
            p = _softmax(scores, axis=-1)
            o = torch.einsum('bhqk,bhke->bhqe', p, v)
            o = torch.einsum('bhte,hed->btd', o, Wo)
            mem = o[:, :M, :].clone()
            outs.append(o[:, M:, :])
        return torch.concatenate(outs, axis=1)
    x = A_x
    g = _sigmoid((x * gate_w).sum(-1, keepdims=True))
    a = _A_ref(x, A_wqkv, A_wo)
    b = _B_ref(x, B_mem0, B_Wq, B_Wk, B_Wv, B_Wo, B_seg_len)
    return g * a + (1 - g) * b
```

```cpp
#include <hip/hip_runtime.h>
#include <hip/hip_bf16.h>
#include <stdint.h>

typedef unsigned u32;
typedef unsigned short u16;
typedef u32 v2u __attribute__((ext_vector_type(2)));
typedef u32 v4u __attribute__((ext_vector_type(4)));
typedef u32 v8u __attribute__((ext_vector_type(8)));
typedef float v2f __attribute__((ext_vector_type(2)));
typedef float v4f __attribute__((ext_vector_type(4)));
typedef short s4v __attribute__((ext_vector_type(4)));
typedef __bf16 b2v __attribute__((ext_vector_type(2)));
typedef __bf16 b8v __attribute__((ext_vector_type(8)));
typedef __bf16 b16v __attribute__((ext_vector_type(16)));
typedef __amdgpu_buffer_rsrc_t rsrc_t;
#define LAS __attribute__((address_space(3)))
#define DEV __device__ __forceinline__

#define TIMED 0

constexpr u32 SENT = 0xFFFFFFFFu;
constexpr u32 MB = 1u << 20;
constexpr u32 R0 = 0, R1 = 2 * MB, WT = 4 * MB, TPM = 18 * MB, OB = 20 * MB, GATE = 22 * MB, KVP = 22 * MB + 65536;
constexpr u32 FLG = 23 * MB, TPF = FLG, OF = FLG + 65536, KF = OF + 8192, LAT = KF + 8192, M0 = LAT + 4096, DBG = FLG + 131072, AT = 26 * MB, XF = 42 * MB, WS_END = 46 * MB;
constexpr u32 TILE = 14336;
constexpr float LOG2E = 1.4426950408889634f;

DEV v4f mfma(v4u a, v4u b, v4f c) { return __builtin_amdgcn_mfma_f32_16x16x32_bf16(__builtin_bit_cast(b8v, a), __builtin_bit_cast(b8v, b), c, 0, 0, 0); }
DEV v4f mfma16(v2u a, v2u b, v4f c) { return __builtin_amdgcn_mfma_f32_16x16x16bf16_1k(__builtin_bit_cast(s4v, a), __builtin_bit_cast(s4v, b), c, 0, 0, 0); }
DEV v4f smf(v4u a, v8u b, v4f c, int idx) { return __builtin_amdgcn_smfmac_f32_16x16x64_bf16(__builtin_bit_cast(b8v, a), __builtin_bit_cast(b16v, b), c, idx, 0, 0); }
DEV v4u lo4(v8u x) { return __builtin_shufflevector(x, x, 0, 1, 2, 3); }
DEV v4u hi4(v8u x) { return __builtin_shufflevector(x, x, 4, 5, 6, 7); }
DEV v8u cat4(v4u a, v4u b) { return __builtin_shufflevector(a, b, 0, 1, 2, 3, 4, 5, 6, 7); }
DEV u32 pk(float a, float b) { return __builtin_bit_cast(u32, __builtin_convertvector((v2f){a, b}, b2v)); }
DEV float rbf(float a) { return __uint_as_float(pk(a, a) << 16); }
DEV u32 nosent(u32 x) { return min(x, 0xFFFFFFFEu); }
DEV u32 max4(v4u x) { return max(max(x.x, x.y), max(x.z, x.w)); }
DEV float ex2(float x) { return __builtin_amdgcn_exp2f(x); }
template <int C> DEV float dpp(float x) { return __int_as_float(__builtin_amdgcn_update_dpp(0, __float_as_int(x), C, 0xf, 0xf, true)); }
constexpr int X1 = 0xB1, X2 = 0x4E, HM = 0x141, RM = 0x140;
DEV float red8max(float x) { x = fmaxf(x, dpp<X1>(x)); x = fmaxf(x, dpp<X2>(x)); return fmaxf(x, dpp<HM>(x)); }
DEV float red8sum(float x) { x += dpp<X1>(x); x += dpp<X2>(x); return x + dpp<HM>(x); }
DEV float red32max(float x) { x = red8max(x); x = fmaxf(x, dpp<RM>(x)); auto r = __builtin_amdgcn_permlane16_swap(__float_as_uint(x), __float_as_uint(x), false, false); return fmaxf(__uint_as_float(r[0]), __uint_as_float(r[1])); }
DEV float red32sum(float x) { x = red8sum(x); x += dpp<RM>(x); auto r = __builtin_amdgcn_permlane16_swap(__float_as_uint(x), __float_as_uint(x), false, false); return __uint_as_float(r[0]) + __uint_as_float(r[1]); }
DEV v4u ld16(rsrc_t r, u32 off) { return __builtin_amdgcn_raw_buffer_load_b128(r, off, 0, 0); }
DEV v4u ld16c(rsrc_t r, u32 off) { return __builtin_amdgcn_raw_buffer_load_b128(r, off, 0, 16); }
DEV u32 ld4c(rsrc_t r, u32 off) { return __builtin_amdgcn_raw_buffer_load_b32(r, off, 0, 16); }
DEV void st16c(rsrc_t r, u32 off, v4u v) { __builtin_amdgcn_raw_buffer_store_b128(v, r, off, 0, 16); }
DEV void st8c(rsrc_t r, u32 off, v2u v) { __builtin_amdgcn_raw_buffer_store_b64(v, r, off, 0, 16); }
DEV void st4c(rsrc_t r, u32 off, u32 v) { __builtin_amdgcn_raw_buffer_store_b32(v, r, off, 0, 16); }
DEV void drain() { asm volatile("s_waitcnt vmcnt(0)" ::: "memory"); }
DEV void bar() { asm volatile("s_waitcnt lgkmcnt(0)\n\ts_barrier" ::: "memory"); }
DEV u32 rt32() { unsigned long long t; asm volatile("s_memrealtime %0\n\ts_waitcnt lgkmcnt(0)" : "=s"(t)::"memory"); return (u32)t; }
DEV u32 clk32() { unsigned long long t; asm volatile("s_memtime %0\n\ts_waitcnt lgkmcnt(0)" : "=s"(t)::"memory"); return (u32)t; }
template <int L> DEV u32 lane(u32 v) { u32 r; asm volatile("s_nop 0\n\tv_readlane_b32 %0, %1, %2\n\ts_nop 1" : "=s"(r) : "v"(v), "n"(L)); return r; }
template <typename T> DEV T lds_r(u32 a) { return *(const LAS T*)(uintptr_t)a; }
template <typename T> DEV void lds_w(u32 a, T v) { *(LAS T*)(uintptr_t)a = v; }
DEV u32 xfrag(int piece) { return XF + piece * 16384 + ((piece >> 7) & 1) * 8192; }
DEV u32 chunk(int s, u32 cc) { return (u32)s * 32768u + ((cc ^ ((u32)(s >> 3) & 2u)) << 13); }

struct Args { const u16 *x, *wqkv, *wo, *mem0, *Wq, *Wk, *Wv, *Wo, *gw; u16* out; char* ws;
};

#define C8(p, n) p #n "0", p #n "1", p #n "2", p #n "3", p #n "4", p #n "5", p #n "6", p #n "7", p #n "8", p #n "9"
#define L_ASM \
    "v_mbcnt_lo_u32_b32 v40, -1, 0\n" \
    "v_mbcnt_hi_u32_b32 v40, -1, v40\n" \
    "v_lshrrev_b32 v42, 4, v40\n" \
    "v_xor_b32 v42, v42, v40\n" \
    "v_and_b32 v42, 7, v42\n" \
    "v_lshlrev_b32 v42, 4, v42\n" \
    "v_lshrrev_b32 v49, 5, v40\n" \
    "v_bfe_u32 v50, v40, 3, 2\n" \
    "v_lshl_add_u32 v49, v49, 3, v50\n" \
    "v_lshl_add_u32 v42, v49, 13, v42\n" \
    "v_xor_b32 v43, 64, v42\n" \
    "v_add_u32 v43, 0x20000, v43\n" \
    "v_add_u32 v44, 0x8000, v42\n" \
    "v_add_u32 v45, 0x8000, v43\n" \
    "s_mov_b64 s[8:9], %0\n" \
    "s_mov_b32 s10, 0x380000\n" \
    "s_mov_b32 s11, 0x20000\n" \
    "s_mov_b32 s12, %1\n" \
    "s_and_b32 s9, s9, 0xffff\n" \
    "s_mov_b32 s13, 0\n" \
    "s_mov_b32 m0, 57344\n" \
    "LKLrow_%=:\n" \
    "s_nop 0\n" \
    "buffer_load_dwordx4 v42, s[8:11], s12 offen lds\n" \
    "s_add_u32 m0, m0, 1024\n" \
    "s_nop 0\n" \
    "buffer_load_dwordx4 v43, s[8:11], s12 offen lds\n" \
    "s_add_u32 m0, m0, 1024\n" \
    "s_nop 0\n" \
    "buffer_load_dwordx4 v44, s[8:11], s12 offen lds\n" \
    "s_add_u32 m0, m0, 1024\n" \
    "s_nop 0\n" \
    "buffer_load_dwordx4 v45, s[8:11], s12 offen lds\n" \
    "s_add_u32 m0, m0, 1024\n" \
    "s_add_u32 s12, s12, 128\n" \
    "s_nop 0\n" \
    "buffer_load_dwordx4 v42, s[8:11], s12 offen lds\n" \
    "s_add_u32 m0, m0, 1024\n" \
    "s_nop 0\n" \
    "buffer_load_dwordx4 v43, s[8:11], s12 offen lds\n" \
    "s_add_u32 m0, m0, 1024\n" \
    "s_nop 0\n" \
    "buffer_load_dwordx4 v44, s[8:11], s12 offen lds\n" \
    "s_add_u32 m0, m0, 1024\n" \
    "s_nop 0\n" \
    "buffer_load_dwordx4 v45, s[8:11], s12 offen lds\n" \
    "s_add_u32 m0, m0, 1024\n" \
    "s_add_u32 s12, s12, 128\n" \
    "s_nop 0\n" \
    "buffer_load_dwordx4 v42, s[8:11], s12 offen lds\n" \
    "s_add_u32 m0, m0, 1024\n" \
    "s_nop 0\n" \
    "buffer_load_dwordx4 v43, s[8:11], s12 offen lds\n" \
    "s_add_u32 m0, m0, 1024\n" \
    "s_nop 0\n" \
    "buffer_load_dwordx4 v44, s[8:11], s12 offen lds\n" \
    "s_add_u32 m0, m0, 1024\n" \
    "s_nop 0\n" \
    "buffer_load_dwordx4 v45, s[8:11], s12 offen lds\n" \
    "s_add_u32 m0, m0, 1024\n" \
    "s_add_u32 s12, s12, 128\n" \
    "s_nop 0\n" \
    "buffer_load_dwordx4 v42, s[8:11], s12 offen lds\n" \
    "s_add_u32 m0, m0, 1024\n" \
    "s_nop 0\n" \
    "buffer_load_dwordx4 v43, s[8:11], s12 offen lds\n" \
    "s_add_u32 m0, m0, 1024\n" \
    "s_nop 0\n" \
    "buffer_load_dwordx4 v44, s[8:11], s12 offen lds\n" \
    "s_add_u32 m0, m0, 1024\n" \
    "s_nop 0\n" \
    "buffer_load_dwordx4 v45, s[8:11], s12 offen lds\n" \
    "s_add_u32 m0, m0, 1024\n" \
    "s_add_u32 s12, s12, 261760\n" \
    "s_add_u32 s13, s13, 1\n" \
    "s_cmp_eq_u32 s13, 6\n" \
    "s_cselect_b32 m0, 57344, m0\n" \
    "s_cmp_lg_u32 s13, 2\n" \
    "s_cbranch_scc1 LKLnone_%=\n" \
    "s_waitcnt vmcnt(16)\n" \
    "s_barrier\n" \
    "LKLnone_%=:\n" \
    "s_cmp_lt_u32 s13, 4\n" \
    "s_cbranch_scc1 LKLrow_%=\n" \
    "s_waitcnt vmcnt(32)\n" \
    "s_barrier\n" \
    "s_cmp_lt_u32 s13, 7\n" \
    "s_cbranch_scc1 LKLrow_%=\n" \
    "s_waitcnt vmcnt(16)\n" \
    "s_barrier\n" \
    "s_waitcnt vmcnt(0)\n" \
    "s_barrier\n"
#define B_ASM \
    "v_mbcnt_lo_u32_b32 v40, -1, 0\n" \
    "v_mbcnt_hi_u32_b32 v40, -1, v40\n" \
    "s_mov_b32 s16, %1\n" \
    "s_mov_b32 s17, %2\n" \
    "s_mov_b32 s19, %3\n" \
    "s_and_b32 s20, s16, 15\n" \
    "s_cmp_gt_u32 s16, 15\n" \
    "s_cbranch_scc1 LKBwo_%=\n" \
    "v_lshrrev_b32 v42, 4, v40\n" \
    "v_xor_b32 v42, v42, v40\n" \
    "v_and_b32 v42, 7, v42\n" \
    "v_lshlrev_b32 v42, 4, v42\n" \
    "v_lshrrev_b32 v49, 5, v40\n" \
    "v_bfe_u32 v50, v40, 3, 2\n" \
    "v_lshl_add_u32 v49, v49, 3, v50\n" \
    "v_lshl_add_u32 v42, v49, 13, v42\n" \
    "s_mul_i32 s26, s17, 7\n" \
    "s_add_u32 s26, s26, s19\n" \
    "s_lshr_b32 s27, s26, 1\n" \
    "s_lshl_b32 s27, s27, 3\n" \
    "s_and_b32 s26, s26, 1\n" \
    "s_add_u32 s27, s27, s26\n" \
    "s_lshl_b32 s27, s27, 15\n" \
    "s_lshl_b32 s26, s20, 9\n" \
    "s_add_u32 s27, s27, s26\n" \
    "v_xor_b32 v43, 64, v42\n" \
    "v_add_u32 v43, 0x20000, v43\n" \
    "s_mov_b32 s28, 128\n" \
    "s_mov_b32 s29, 1024\n" \
    "s_lshl_b32 m0, s19, 13\n" \
    "s_branch LKBgo_%=\n" \
    "LKBwo_%=:\n" \
    "s_lshl_b32 s26, s19, 6\n" \
    "v_add_u32 v49, s26, v40\n" \
    "v_mul_u32_u24 v50, 4682, v49\n" \
    "v_lshrrev_b32 v50, 16, v50\n" \
    "v_mul_u32_u24 v51, 14, v50\n" \
    "v_sub_u32 v49, v49, v51\n" \
    "v_and_b32 v51, 3, v50\n" \
    "v_lshrrev_b32 v52, 4, v50\n" \
    "v_lshl_add_u32 v51, v52, 2, v51\n" \
    "v_bfe_u32 v52, v50, 2, 2\n" \
    "v_lshl_add_u32 v51, v52, 3, v51\n" \
    "v_mul_u32_u24 v51, 896, v51\n" \
    "v_lshl_add_u32 v42, v49, 4, v51\n" \
    "v_add_u32 v43, 28672, v42\n" \
    "s_mul_i32 s26, s20, 229376\n" \
    "s_mul_i32 s27, s17, 224\n" \
    "s_add_u32 s27, s26, s27\n" \
    "s_mov_b32 s28, 57344\n" \
    "s_mov_b32 s29, 7168\n" \
    "s_lshl_b32 m0, s19, 10\n" \
    "LKBgo_%=:\n" \
    "s_mov_b64 s[12:13], %0\n" \
    "s_mov_b32 s14, 0x380000\n" \
    "s_mov_b32 s15, 0x20000\n" \
    "s_and_b32 s13, s13, 0xffff\n" \
    "buffer_load_dwordx4 v42, s[12:15], s27 offen lds\n" \
    "s_add_u32 m0, m0, s29\n" \
    "s_nop 0\n" \
    "buffer_load_dwordx4 v43, s[12:15], s27 offen lds\n" \
    "s_add_u32 m0, m0, s29\n" \
    "s_add_u32 s27, s27, s28\n" \
    "buffer_load_dwordx4 v42, s[12:15], s27 offen lds\n" \
    "s_add_u32 m0, m0, s29\n" \
    "s_nop 0\n" \
    "buffer_load_dwordx4 v43, s[12:15], s27 offen lds\n" \
    "s_add_u32 m0, m0, s29\n" \
    "s_add_u32 s27, s27, s28\n" \
    "buffer_load_dwordx4 v42, s[12:15], s27 offen lds\n" \
    "s_add_u32 m0, m0, s29\n" \
    "s_nop 0\n" \
    "buffer_load_dwordx4 v43, s[12:15], s27 offen lds\n" \
    "s_add_u32 m0, m0, s29\n" \
    "s_add_u32 s27, s27, s28\n" \
    "buffer_load_dwordx4 v42, s[12:15], s27 offen lds\n" \
    "s_add_u32 m0, m0, s29\n" \
    "s_nop 0\n" \
    "buffer_load_dwordx4 v43, s[12:15], s27 offen lds\n" \
    "s_add_u32 m0, m0, s29\n" \
    "s_add_u32 s27, s27, s28\n"
#define K_ASM \
    "s_mov_b64 s[4:5], %0\n" \
    "s_mov_b32 s6, %5\n" \
    "s_mov_b32 s7, 0x20000\n" \
    "s_mov_b32 s16, %1\n" \
    "s_sub_u32 s22, %2, 1024\n" \
    "s_sub_u32 s23, %3, 1024\n" \
    "s_mov_b32 s19, %4\n" \
    "s_and_b32 s5, s5, 0xffff\n" \
    "v_mbcnt_lo_u32_b32 v0, -1, 0\n" \
    "v_mbcnt_hi_u32_b32 v0, -1, v0\n" \
    "v_lshlrev_b32 v1, 4, v0\n" \
    "v_mov_b32 v2, 0x40000000\n" \
    "v_mov_b32 v3, v2\n" \
    "s_cmp_eq_u32 s19, 3\n" \
    "s_cselect_b32 s24, 0x40000000, 0\n" \
    "s_cselect_b32 s25, 0, 1\n" \
    "v_bfe_u32 v8, v0, 1, 3\n" \
    "v_lshrrev_b32 v9, 4, v0\n" \
    "v_xor_b32 v8, v8, v9\n" \
    "v_and_b32 v9, 15, v0\n" \
    "v_lshl_add_u32 v8, v9, 3, v8\n" \
    "v_lshlrev_b32 v6, 4, v8\n" \
    "v_xor_b32 v7, 64, v6\n" \
    "s_lshl_b32 s26, s19, 14\n" \
    "v_add_u32 v10, s26, v6\n" \
    "v_add_u32 v11, s26, v7\n" \
    "s_lshl_b32 s26, s25, 13\n" \
    "v_add_u32 v12, s26, v10\n" \
    "v_add_u32 v13, s26, v11\n" \
    "v_lshrrev_b32 v8, 2, v0\n" \
    "v_mul_u32_u24 v8, 224, v8\n" \
    "v_and_b32 v9, 3, v0\n" \
    "v_lshl_add_u32 v8, v9, 3, v8\n" \
    "s_lshl_b32 s26, s19, 6\n" \
    "v_add_u32 v8, s26, v8\n" \
    "s_lshl_b32 s26, s25, 5\n" \
    "v_add_u32 v9, s26, v8\n" \
    "s_mov_b32 s30, 57344\n" \
    "v_add_u32 v4, s30, v6\n" \
    "v_add_u32 v5, s30, v7\n" \
    "s_mov_b32 s21, 0\n" \
    "s_cmp_gt_u32 s16, 15\n" \
    "s_cselect_b32 s35, 1.0, 0x3db8aa3b\n" \
    "s_waitcnt vmcnt(0)\n" \
    "s_barrier\n" \
    "s_cbranch_scc1 LKKwo_%=\n" \
    "ds_read_b128 v[88:91], v10 offset:0\n" \
    "ds_read_b128 v[92:95], v12 offset:0\n" \
    "ds_read_b128 v[96:99], v11 offset:0\n" \
    "ds_read_b128 v[100:103], v13 offset:0\n" \
    "ds_read_b128 v[104:107], v10 offset:2048\n" \
    "ds_read_b128 v[108:111], v12 offset:2048\n" \
    "ds_read_b128 v[112:115], v11 offset:2048\n" \
    "ds_read_b128 v[116:119], v13 offset:2048\n" \
    "ds_read_b128 v[120:123], v10 offset:4096\n" \
    "ds_read_b128 v[124:127], v12 offset:4096\n" \
    "ds_read_b128 v[128:131], v11 offset:4096\n" \
    "ds_read_b128 v[132:135], v13 offset:4096\n" \
    "s_waitcnt lgkmcnt(6)\n" \
    "ds_read_b128 v[136:139], v10 offset:6144\n" \
    "ds_read_b128 v[140:143], v12 offset:6144\n" \
    "ds_read_b128 v[144:147], v11 offset:6144\n" \
    "ds_read_b128 v[148:151], v13 offset:6144\n" \
    "s_branch LKKfirst_%=\n" \
    "LKKwo_%=:\n" \
    "ds_read_b64_tr_b16 v[88:89], v8 offset:0\n" \
    "ds_read_b64_tr_b16 v[90:91], v8 offset:3584\n" \
    "ds_read_b64_tr_b16 v[92:93], v9 offset:0\n" \
    "ds_read_b64_tr_b16 v[94:95], v9 offset:3584\n" \
    "ds_read_b64_tr_b16 v[96:97], v8 offset:7168\n" \
    "ds_read_b64_tr_b16 v[98:99], v8 offset:10752\n" \
    "ds_read_b64_tr_b16 v[100:101], v9 offset:7168\n" \
    "ds_read_b64_tr_b16 v[102:103], v9 offset:10752\n" \
    "ds_read_b64_tr_b16 v[104:105], v8 offset:14336\n" \
    "ds_read_b64_tr_b16 v[106:107], v8 offset:17920\n" \
    "ds_read_b64_tr_b16 v[108:109], v9 offset:14336\n" \
    "ds_read_b64_tr_b16 v[110:111], v9 offset:17920\n" \
    "s_waitcnt lgkmcnt(8)\n" \
    "ds_read_b64_tr_b16 v[112:113], v8 offset:21504\n" \
    "ds_read_b64_tr_b16 v[114:115], v8 offset:25088\n" \
    "ds_read_b64_tr_b16 v[116:117], v9 offset:21504\n" \
    "ds_read_b64_tr_b16 v[118:119], v9 offset:25088\n" \
    "s_waitcnt lgkmcnt(8)\n" \
    "ds_read_b64_tr_b16 v[120:121], v8 offset:28672\n" \
    "ds_read_b64_tr_b16 v[122:123], v8 offset:32256\n" \
    "ds_read_b64_tr_b16 v[124:125], v9 offset:28672\n" \
    "ds_read_b64_tr_b16 v[126:127], v9 offset:32256\n" \
    "s_waitcnt lgkmcnt(8)\n" \
    "ds_read_b64_tr_b16 v[128:129], v8 offset:35840\n" \
    "ds_read_b64_tr_b16 v[130:131], v8 offset:39424\n" \
    "ds_read_b64_tr_b16 v[132:133], v9 offset:35840\n" \
    "ds_read_b64_tr_b16 v[134:135], v9 offset:39424\n" \
    "s_waitcnt lgkmcnt(8)\n" \
    "ds_read_b64_tr_b16 v[136:137], v8 offset:43008\n" \
    "ds_read_b64_tr_b16 v[138:139], v8 offset:46592\n" \
    "ds_read_b64_tr_b16 v[140:141], v9 offset:43008\n" \
    "ds_read_b64_tr_b16 v[142:143], v9 offset:46592\n" \
    "s_waitcnt lgkmcnt(8)\n" \
    "ds_read_b64_tr_b16 v[144:145], v8 offset:50176\n" \
    "ds_read_b64_tr_b16 v[146:147], v8 offset:53760\n" \
    "ds_read_b64_tr_b16 v[148:149], v9 offset:50176\n" \
    "ds_read_b64_tr_b16 v[150:151], v9 offset:53760\n" \
    "LKKfirst_%=:\n" \
    "s_waitcnt lgkmcnt(6)\n" \
    "ds_read_b128 v[24:27], v4 offset:0\n" \
    "ds_read_b128 v[28:31], v4 offset:2048\n" \
    "ds_read_b128 v[32:35], v5 offset:0\n" \
    "ds_read_b128 v[36:39], v5 offset:2048\n" \
    "ds_read_b128 v[40:43], v4 offset:4096\n" \
    "ds_read_b128 v[44:47], v4 offset:6144\n" \
    "ds_read_b128 v[48:51], v5 offset:4096\n" \
    "ds_read_b128 v[52:55], v5 offset:6144\n" \
    "LKKrow_%=:\n" \
    "s_waitcnt lgkmcnt(6)\n" \
    "v_mfma_f32_16x16x32_bf16 v[152:155], v[24:27], v[88:91], 0\n" \
    "s_add_u32 s21, s21, 1\n" \
    "s_add_u32 s30, s30, 16384\n" \
    "v_mfma_f32_16x16x32_bf16 v[160:163], v[24:27], v[92:95], 0\n" \
    "ds_read_b128 v[56:59], v4 offset:8192\n" \
    "s_cmp_eq_u32 s21, 6\n" \
    "v_mfma_f32_16x16x32_bf16 v[156:159], v[28:31], v[88:91], 0\n" \
    "s_cselect_b32 s30, 57344, s30\n" \
    "v_mul_f32 v168, s35, v168\n" \
    "v_mfma_f32_16x16x32_bf16 v[164:167], v[28:31], v[92:95], 0\n" \
    "ds_read_b128 v[60:63], v4 offset:10240\n" \
    "s_waitcnt lgkmcnt(6)\n" \
    "v_mfma_f32_16x16x32_bf16 v[152:155], v[32:35], v[96:99], v[152:155]\n" \
    "v_mul_f32 v169, s35, v169\n" \
    "v_mul_f32 v170, s35, v170\n" \
    "v_mfma_f32_16x16x32_bf16 v[160:163], v[32:35], v[100:103], v[160:163]\n" \
    "ds_read_b128 v[64:67], v5 offset:8192\n" \
    "v_mul_f32 v171, s35, v171\n" \
    "v_mfma_f32_16x16x32_bf16 v[156:159], v[36:39], v[96:99], v[156:159]\n" \
    "v_mul_f32 v172, s35, v172\n" \
    "v_mul_f32 v173, s35, v173\n" \
    "v_mfma_f32_16x16x32_bf16 v[164:167], v[36:39], v[100:103], v[164:167]\n" \
    "ds_read_b128 v[68:71], v5 offset:10240\n" \
    "s_waitcnt lgkmcnt(6)\n" \
    "v_mfma_f32_16x16x32_bf16 v[152:155], v[40:43], v[104:107], v[152:155]\n" \
    "v_mul_f32 v174, s35, v174\n" \
    "v_mul_f32 v175, s35, v175\n" \
    "v_mfma_f32_16x16x32_bf16 v[160:163], v[40:43], v[108:111], v[160:163]\n" \
    "ds_read_b128 v[72:75], v4 offset:12288\n" \
    "v_cvt_pk_bf16_f32 v16, v168, v169\n" \
    "v_mfma_f32_16x16x32_bf16 v[156:159], v[44:47], v[104:107], v[156:159]\n" \
    "v_cvt_pk_bf16_f32 v17, v170, v171\n" \
    "v_cvt_pk_bf16_f32 v18, v172, v173\n" \
    "v_mfma_f32_16x16x32_bf16 v[164:167], v[44:47], v[108:111], v[164:167]\n" \
    "ds_read_b128 v[76:79], v4 offset:14336\n" \
    "s_waitcnt lgkmcnt(6)\n" \
    "v_mfma_f32_16x16x32_bf16 v[152:155], v[48:51], v[112:115], v[152:155]\n" \
    "v_cvt_pk_bf16_f32 v19, v174, v175\n" \
    "buffer_store_dwordx4 v[16:19], v2, s[4:7], s22 offen sc1\n" \
    "v_mfma_f32_16x16x32_bf16 v[160:163], v[48:51], v[116:119], v[160:163]\n" \
    "ds_read_b128 v[80:83], v5 offset:12288\n" \
    "s_add_u32 s22, s22, 1024\n" \
    "v_mfma_f32_16x16x32_bf16 v[156:159], v[52:55], v[112:115], v[156:159]\n" \
    "v_mul_f32 v176, s35, v176\n" \
    "v_mul_f32 v177, s35, v177\n" \
    "v_mfma_f32_16x16x32_bf16 v[164:167], v[52:55], v[116:119], v[164:167]\n" \
    "ds_read_b128 v[84:87], v5 offset:14336\n" \
    "s_waitcnt lgkmcnt(6)\n" \
    "v_mfma_f32_16x16x32_bf16 v[152:155], v[56:59], v[120:123], v[152:155]\n" \
    "s_cmp_eq_u32 s21, 7\n" \
    "s_cbranch_scc1 LKKlast_%=\n" \
    "s_barrier\n" \
    "LKKlast_%=:\n" \
    "v_add_u32 v4, s30, v6\n" \
    "v_add_u32 v5, s30, v7\n" \
    "v_mfma_f32_16x16x32_bf16 v[160:163], v[56:59], v[124:127], v[160:163]\n" \
    "ds_read_b128 v[24:27], v4 offset:0\n" \
    "v_mul_f32 v178, s35, v178\n" \
    "v_mfma_f32_16x16x32_bf16 v[156:159], v[60:63], v[120:123], v[156:159]\n" \
    "v_mul_f32 v179, s35, v179\n" \
    "v_mul_f32 v180, s35, v180\n" \
    "v_mfma_f32_16x16x32_bf16 v[164:167], v[60:63], v[124:127], v[164:167]\n" \
    "ds_read_b128 v[28:31], v4 offset:2048\n" \
    "s_waitcnt lgkmcnt(6)\n" \
    "v_mfma_f32_16x16x32_bf16 v[152:155], v[64:67], v[128:131], v[152:155]\n" \
    "v_mul_f32 v181, s35, v181\n" \
    "v_mul_f32 v182, s35, v182\n" \
    "v_mfma_f32_16x16x32_bf16 v[160:163], v[64:67], v[132:135], v[160:163]\n" \
    "ds_read_b128 v[32:35], v5 offset:0\n" \
    "v_mul_f32 v183, s35, v183\n" \
    "v_mfma_f32_16x16x32_bf16 v[156:159], v[68:71], v[128:131], v[156:159]\n" \
    "v_cvt_pk_bf16_f32 v20, v176, v177\n" \
    "v_cvt_pk_bf16_f32 v21, v178, v179\n" \
    "v_mfma_f32_16x16x32_bf16 v[164:167], v[68:71], v[132:135], v[164:167]\n" \
    "ds_read_b128 v[36:39], v5 offset:2048\n" \
    "s_waitcnt lgkmcnt(6)\n" \
    "v_mfma_f32_16x16x32_bf16 v[152:155], v[72:75], v[136:139], v[152:155]\n" \
    "v_cvt_pk_bf16_f32 v22, v180, v181\n" \
    "v_cvt_pk_bf16_f32 v23, v182, v183\n" \
    "v_mfma_f32_16x16x32_bf16 v[160:163], v[72:75], v[140:143], v[160:163]\n" \
    "ds_read_b128 v[40:43], v4 offset:4096\n" \
    "buffer_store_dwordx4 v[20:23], v3, s[4:7], s23 offen sc1\n" \
    "v_mfma_f32_16x16x32_bf16 v[156:159], v[76:79], v[136:139], v[156:159]\n" \
    "s_add_u32 s23, s23, 1024\n" \
    "v_mov_b32 v2, v1\n" \
    "v_mfma_f32_16x16x32_bf16 v[164:167], v[76:79], v[140:143], v[164:167]\n" \
    "ds_read_b128 v[44:47], v4 offset:6144\n" \
    "s_waitcnt lgkmcnt(6)\n" \
    "v_mfma_f32_16x16x32_bf16 v[152:155], v[80:83], v[144:147], v[152:155]\n" \
    "v_or_b32 v3, s24, v1\n" \
    "v_mfma_f32_16x16x32_bf16 v[160:163], v[80:83], v[148:151], v[160:163]\n" \
    "ds_read_b128 v[48:51], v5 offset:4096\n" \
    "v_mfma_f32_16x16x32_bf16 v[156:159], v[84:87], v[144:147], v[156:159]\n" \
    "v_mfma_f32_16x16x32_bf16 v[164:167], v[84:87], v[148:151], v[164:167]\n" \
    "ds_read_b128 v[52:55], v5 offset:6144\n" \
    "s_cmp_eq_u32 s21, 7\n" \
    "s_cbranch_scc1 LKKdone_%=\n" \
    "s_waitcnt lgkmcnt(6)\n" \
    "v_mfma_f32_16x16x32_bf16 v[168:171], v[24:27], v[88:91], 0\n" \
    "s_add_u32 s21, s21, 1\n" \
    "s_add_u32 s30, s30, 16384\n" \
    "v_mfma_f32_16x16x32_bf16 v[176:179], v[24:27], v[92:95], 0\n" \
    "ds_read_b128 v[56:59], v4 offset:8192\n" \
    "s_cmp_eq_u32 s21, 6\n" \
    "v_mfma_f32_16x16x32_bf16 v[172:175], v[28:31], v[88:91], 0\n" \
    "s_cselect_b32 s30, 57344, s30\n" \
    "v_mul_f32 v152, s35, v152\n" \
    "v_mfma_f32_16x16x32_bf16 v[180:183], v[28:31], v[92:95], 0\n" \
    "ds_read_b128 v[60:63], v4 offset:10240\n" \
    "s_waitcnt lgkmcnt(6)\n" \
    "v_mfma_f32_16x16x32_bf16 v[168:171], v[32:35], v[96:99], v[168:171]\n" \
    "v_mul_f32 v153, s35, v153\n" \
    "v_mul_f32 v154, s35, v154\n" \
    "v_mfma_f32_16x16x32_bf16 v[176:179], v[32:35], v[100:103], v[176:179]\n" \
    "ds_read_b128 v[64:67], v5 offset:8192\n" \
    "v_mul_f32 v155, s35, v155\n" \
    "v_mfma_f32_16x16x32_bf16 v[172:175], v[36:39], v[96:99], v[172:175]\n" \
    "v_mul_f32 v156, s35, v156\n" \
    "v_mul_f32 v157, s35, v157\n" \
    "v_mfma_f32_16x16x32_bf16 v[180:183], v[36:39], v[100:103], v[180:183]\n" \
    "ds_read_b128 v[68:71], v5 offset:10240\n" \
    "s_waitcnt lgkmcnt(6)\n" \
    "v_mfma_f32_16x16x32_bf16 v[168:171], v[40:43], v[104:107], v[168:171]\n" \
    "v_mul_f32 v158, s35, v158\n" \
    "v_mul_f32 v159, s35, v159\n" \
    "v_mfma_f32_16x16x32_bf16 v[176:179], v[40:43], v[108:111], v[176:179]\n" \
    "ds_read_b128 v[72:75], v4 offset:12288\n" \
    "v_cvt_pk_bf16_f32 v16, v152, v153\n" \
    "v_mfma_f32_16x16x32_bf16 v[172:175], v[44:47], v[104:107], v[172:175]\n" \
    "v_cvt_pk_bf16_f32 v17, v154, v155\n" \
    "v_cvt_pk_bf16_f32 v18, v156, v157\n" \
    "v_mfma_f32_16x16x32_bf16 v[180:183], v[44:47], v[108:111], v[180:183]\n" \
    "ds_read_b128 v[76:79], v4 offset:14336\n" \
    "s_waitcnt lgkmcnt(6)\n" \
    "v_mfma_f32_16x16x32_bf16 v[168:171], v[48:51], v[112:115], v[168:171]\n" \
    "v_cvt_pk_bf16_f32 v19, v158, v159\n" \
    "buffer_store_dwordx4 v[16:19], v2, s[4:7], s22 offen sc1\n" \
    "v_mfma_f32_16x16x32_bf16 v[176:179], v[48:51], v[116:119], v[176:179]\n" \
    "ds_read_b128 v[80:83], v5 offset:12288\n" \
    "s_add_u32 s22, s22, 1024\n" \
    "v_mfma_f32_16x16x32_bf16 v[172:175], v[52:55], v[112:115], v[172:175]\n" \
    "v_mul_f32 v160, s35, v160\n" \
    "v_mul_f32 v161, s35, v161\n" \
    "v_mfma_f32_16x16x32_bf16 v[180:183], v[52:55], v[116:119], v[180:183]\n" \
    "ds_read_b128 v[84:87], v5 offset:14336\n" \
    "s_waitcnt lgkmcnt(6)\n" \
    "v_mfma_f32_16x16x32_bf16 v[168:171], v[56:59], v[120:123], v[168:171]\n" \
    "s_barrier\n" \
    "v_add_u32 v4, s30, v6\n" \
    "v_add_u32 v5, s30, v7\n" \
    "v_mfma_f32_16x16x32_bf16 v[176:179], v[56:59], v[124:127], v[176:179]\n" \
    "ds_read_b128 v[24:27], v4 offset:0\n" \
    "v_mul_f32 v162, s35, v162\n" \
    "v_mfma_f32_16x16x32_bf16 v[172:175], v[60:63], v[120:123], v[172:175]\n" \
    "v_mul_f32 v163, s35, v163\n" \
    "v_mul_f32 v164, s35, v164\n" \
    "v_mfma_f32_16x16x32_bf16 v[180:183], v[60:63], v[124:127], v[180:183]\n" \
    "ds_read_b128 v[28:31], v4 offset:2048\n" \
    "s_waitcnt lgkmcnt(6)\n" \
    "v_mfma_f32_16x16x32_bf16 v[168:171], v[64:67], v[128:131], v[168:171]\n" \
    "v_mul_f32 v165, s35, v165\n" \
    "v_mul_f32 v166, s35, v166\n" \
    "v_mfma_f32_16x16x32_bf16 v[176:179], v[64:67], v[132:135], v[176:179]\n" \
    "ds_read_b128 v[32:35], v5 offset:0\n" \
    "v_mul_f32 v167, s35, v167\n" \
    "v_mfma_f32_16x16x32_bf16 v[172:175], v[68:71], v[128:131], v[172:175]\n" \
    "v_cvt_pk_bf16_f32 v20, v160, v161\n" \
    "v_cvt_pk_bf16_f32 v21, v162, v163\n" \
    "v_mfma_f32_16x16x32_bf16 v[180:183], v[68:71], v[132:135], v[180:183]\n" \
    "ds_read_b128 v[36:39], v5 offset:2048\n" \
    "s_waitcnt lgkmcnt(6)\n" \
    "v_mfma_f32_16x16x32_bf16 v[168:171], v[72:75], v[136:139], v[168:171]\n" \
    "v_cvt_pk_bf16_f32 v22, v164, v165\n" \
    "v_cvt_pk_bf16_f32 v23, v166, v167\n" \
    "v_mfma_f32_16x16x32_bf16 v[176:179], v[72:75], v[140:143], v[176:179]\n" \
    "ds_read_b128 v[40:43], v4 offset:4096\n" \
    "buffer_store_dwordx4 v[20:23], v3, s[4:7], s23 offen sc1\n" \
    "v_mfma_f32_16x16x32_bf16 v[172:175], v[76:79], v[136:139], v[172:175]\n" \
    "s_add_u32 s23, s23, 1024\n" \
    "v_mov_b32 v2, v1\n" \
    "v_mfma_f32_16x16x32_bf16 v[180:183], v[76:79], v[140:143], v[180:183]\n" \
    "ds_read_b128 v[44:47], v4 offset:6144\n" \
    "s_waitcnt lgkmcnt(6)\n" \
    "v_mfma_f32_16x16x32_bf16 v[168:171], v[80:83], v[144:147], v[168:171]\n" \
    "v_or_b32 v3, s24, v1\n" \
    "v_mfma_f32_16x16x32_bf16 v[176:179], v[80:83], v[148:151], v[176:179]\n" \
    "ds_read_b128 v[48:51], v5 offset:4096\n" \
    "v_mfma_f32_16x16x32_bf16 v[172:175], v[84:87], v[144:147], v[172:175]\n" \
    "v_mfma_f32_16x16x32_bf16 v[180:183], v[84:87], v[148:151], v[180:183]\n" \
    "ds_read_b128 v[52:55], v5 offset:6144\n" \
    "s_branch LKKrow_%=\n" \
    "LKKdone_%=:\n" \
    "s_nop 7\n" \
    "v_mul_f32 v152, s35, v152\n" \
    "v_mul_f32 v153, s35, v153\n" \
    "v_mul_f32 v154, s35, v154\n" \
    "v_mul_f32 v155, s35, v155\n" \
    "v_mul_f32 v156, s35, v156\n" \
    "v_mul_f32 v157, s35, v157\n" \
    "v_mul_f32 v158, s35, v158\n" \
    "v_mul_f32 v159, s35, v159\n" \
    "v_cvt_pk_bf16_f32 v16, v152, v153\n" \
    "v_cvt_pk_bf16_f32 v17, v154, v155\n" \
    "v_cvt_pk_bf16_f32 v18, v156, v157\n" \
    "v_cvt_pk_bf16_f32 v19, v158, v159\n" \
    "buffer_store_dwordx4 v[16:19], v2, s[4:7], s22 offen sc1\n" \
    "s_add_u32 s22, s22, 1024\n" \
    "v_mul_f32 v160, s35, v160\n" \
    "v_mul_f32 v161, s35, v161\n" \
    "v_mul_f32 v162, s35, v162\n" \
    "v_mul_f32 v163, s35, v163\n" \
    "v_mul_f32 v164, s35, v164\n" \
    "v_mul_f32 v165, s35, v165\n" \
    "v_mul_f32 v166, s35, v166\n" \
    "v_mul_f32 v167, s35, v167\n" \
    "v_cvt_pk_bf16_f32 v20, v160, v161\n" \
    "v_cvt_pk_bf16_f32 v21, v162, v163\n" \
    "v_cvt_pk_bf16_f32 v22, v164, v165\n" \
    "v_cvt_pk_bf16_f32 v23, v166, v167\n" \
    "buffer_store_dwordx4 v[20:23], v3, s[4:7], s23 offen sc1\n" \
    "s_add_u32 s23, s23, 1024\n" \
    "s_waitcnt lgkmcnt(0)\n"
#define FARMASK 36
#define MB0 0
#define PREF 0
#define LDEL 0
#define NPROBE 3
__global__ void __launch_bounds__(512) k_prep(Args a) {
    extern __shared__ __attribute__((aligned(16))) char smem[];
    const int b = blockIdx.x, x = b & 7, j = b >> 3, l = threadIdx.x & 63, w = __builtin_amdgcn_readfirstlane(threadIdx.x >> 6);
    const int c = l & 15, g = l >> 4;
    const rsrc_t ws = __builtin_amdgcn_make_buffer_rsrc(a.ws, 0, WS_END, 0x00020000);
    const int mi = (j & 16) + x * 2 + ((j >> 3) & 1);
    const u32 own = (b & 1) * R1 + chunk(b >> 2, b & 3);
    if (w == 7) {
        const u32 off = ((j >> 2) & 1) * R1 + chunk(2 * mi, j & 3) + ((b * 5) & 7) * 256;
        st4c(ws, off + (l & 3) * 2048, SENT);
        if (LDEL) __builtin_amdgcn_s_sleep(LDEL);
        asm volatile(L_ASM ::"s"(mi < 16 ? a.Wk : a.Wv), "s"(((j >> 2) & 1) * (7 << 18) + (mi & 15) * 512), "s"((j & 3) * 128)
                     : "memory", "scc", "s8", "s9", "s10", "s11", "s12", "s13", "s14", "s15", C8("v", 4), "v50");
        const u32 xcc = (u32)__builtin_amdgcn_s_getreg(20 | (3 << 11)) & 7u;
        u32 best = 100000;
#pragma unroll
        for (int r = 0; r < NPROBE; ++r) {
            unsigned long long t0, t1; u32 v;
            asm volatile("s_memtime %0\n\ts_load_dword %2, %3, %4\n\ts_waitcnt lgkmcnt(0)\n\ts_memtime %1\n\ts_waitcnt lgkmcnt(0)" : "=&s"(t0), "=&s"(t1), "=&s"(v) : "s"(a.ws + off), "n"(r * 2048) : "memory");
            best = min(best, (u32)t1 - (u32)t0);
        }
        st4c(ws, off + (l & 3) * 2048, SENT);
        if (l == 0) st4c(ws, LAT + (x * 32 + j) * 4, best << 2 | ((j ^ (j >> 2 & 1) ^ (0x7722 >> (2 * xcc))) & 3));
        if (b == 0 && l == 0) st4c(ws, LAT + 1024, xcc);
        return;
    }
    asm volatile(B_ASM ::"s"(mi < 16 ? a.Wq : a.Wo), "s"(mi), "s"(j & 3), "s"(w) : "memory", "scc", C8("s", 1), C8("s", 2), C8("v", 4), "v50", "v51", "v52");
    if (w >= 4) {
        v4u xt[3];
#pragma unroll
        for (int i = 0; i < 3; ++i) xt[i] = *(const v4u*)(a.x + ((b >> 1) * 16 + c) * 448 + ((b & 1) * 7 + min(w - 4 + 3 * i, 6)) * 32 + g * 8);
        v4u m0 = {0, 0, 0, 0};
        if (w == 6 && b >= 80 && b < 87) {
            const int G = (b - 80) * 4 + g, m = c >> 1, hf = c & 1;
            const u32* p = (const u32*)(a.mem0 + m * 448 + G * 16 + 2 * hf);
            m0 = (v4u){nosent(p[0]), nosent(p[2]), nosent(p[4]), nosent(p[6])};
        }
        asm volatile("s_waitcnt vmcnt(3)" ::: "memory");
#pragma unroll
        for (int i = 0; i < 7; ++i) {
            if (i >= MB0) __builtin_amdgcn_s_barrier();
            if (i == MB0) asm volatile("" ::"v"(xt[0]), "v"(xt[1]), "v"(xt[2]));
            if (i < 3) { if (w - 4 + 3 * i < 8) st16c(ws, own + (w - 4 + 3 * i) * 1024 + l * 16, (v4u){SENT, SENT, SENT, SENT}); }
            else if (i < 6) { if (w - 4 + 3 * (i - 3) < 7) st16c(ws, xfrag(b) + (w - 4 + 3 * (i - 3)) * 1024 + l * 16, xt[i - 3]); }
            if (i == 2 && w == 6 && b >= 96) {
                const int u = b - 96, sg = 60 + u / 40, r = u % 40;
                st16c(ws, (r < 32 ? ((r >> 3) & 1) * R1 + chunk(sg, (r >> 3) ^ 1) : TPM + sg * 16384) + (r & 7) * 1024 + l * 16, (v4u){SENT, SENT, SENT, SENT});
            }
            if (i == 2 && w == 6 && b < 87) st16c(ws, (b < 80 ? FLG + b * 1024 : M0 + (b - 80) * 1024) + l * 16, m0);
        }
#pragma unroll
        for (int i = 0; i < MB0; ++i) __builtin_amdgcn_s_barrier();
        return;
    }
    u32 dst[2];
#pragma unroll
    for (int n = 0; n < 2; ++n) {
        const int q = min(2 * w + n, 6);
        const u32 at = AT + (mi * 8 + (j & 7)) * 131072 + q * 16384;
        dst[n] = mi < 16 ? at + (((at >> 21) ^ (FARMASK >> q)) & 1) * 8192 : WT + (mi * 28 + (j & 3) * 7 + q) * TILE + ((j >> 2) & 1) * 7168;
    }
    asm volatile(K_ASM ::"s"(a.ws), "s"(mi), "s"(dst[0]), "s"(dst[1]), "s"(w), "i"(WS_END), "s"(mi * 8 + (j & 7))
                 : "memory", "scc", "vcc", "s4", "s5", "s6", "s7", C8("s", 1), C8("s", 2), "s30", "s35", C8("s", 4), C8("s", 5),
                   "v0", "v1", "v2", "v3", "v4", "v5", "v6", "v7", "v8", "v9", C8("v", 1), C8("v", 2), C8("v", 3), C8("v", 4), C8("v", 5), C8("v", 6), C8("v", 7), C8("v", 8), C8("v", 9), C8("v", 10), C8("v", 11),
                   C8("v", 12), C8("v", 13), C8("v", 14), C8("v", 15), C8("v", 16), C8("v", 17), C8("v", 18));
}

struct Cx { rsrc_t ws; u32 sb, c0, c1; int l, w, budget; };
DEV u32 a_memS(const Cx& k, int s) { return s ? R0 + chunk(s, k.c0) : M0; }
DEV u32 a_memO(const Cx& k, int s) { return s ? R1 + chunk(s, k.c1) : M0; }
DEV u32 a_sp(const Cx& k, int s) { return R0 + chunk(s, k.c0 ^ 2); }
DEV u32 a_ex(const Cx& k, int s) { return R1 + chunk(s, k.c1 ^ 2); }
DEV u32 a_etaS(const Cx& k, int s) { return R0 + chunk(s, k.c0) + 7168; }
DEV u32 a_etaF(const Cx& k, int s) { return R1 + chunk(s, k.c1) + 7168; }
DEV u32 wait_words(Cx& k, u32 off, u32 no, bool slow) {
    u32 v;
#pragma nounroll
    for (;;) {
        asm volatile("" ::: "memory");
        v = ld4c(k.ws, off);
        if (!__any(v == no) || --k.budget < 0) break;
        if (slow) __builtin_amdgcn_s_sleep(4);
    }
    asm volatile("" ::: "memory");
    return v;
}
#define TI(x) "i"(x)
#define T_DS 1078
#define T_DEL 145
#define T_D2 775
#define T_EX 1400
#define T_MO 215
#define T_LATE 950
#define T_PAD 352
#define T_THR 0x42700000
#define T_K2 5
#define T_WSOFF 0
#define T_RD 1250
#define T_RES 550
#define T_DRIFT 140
#define S_ASM \
    "s_mov_b32 s80, %8\n" \
    "s_mov_b32 s81, %9\n" \
    "s_mov_b32 s82, %10\n" \
    "s_mov_b32 s83, %11\n" \
    "s_mov_b32 s84, %12\n" \
    "s_mov_b32 s85, %13\n" \
    "s_mov_b32 s86, %14\n" \
    "s_mov_b32 s66, %15\n" \
    "s_mov_b32 s67, %16\n" \
    "s_mov_b64 s[4:5], %0\n" \
    "s_mov_b32 s6, %7\n" \
    "s_mov_b32 s7, 0x20000\n" \
    "s_mov_b32 s12, %2\n" \
    "s_mov_b32 s13, %3\n" \
    "s_mov_b32 s14, %4\n" \
    "s_mov_b32 s15, %5\n" \
    "s_mov_b32 s16, %6\n" \
    "s_and_b32 s5, s5, 0xffff\n" \
    "s_mov_b32 s8, 40960\n" \
    "s_mov_b32 s9, 69632\n" \
    "s_mov_b32 s17, 300000\n" \
    "s_mov_b32 s19, s82\n" \
    "s_mov_b32 s39, 0\n" \
    "s_mul_i32 s20, s14, 7\n" \
    "s_lshr_b32 s20, s20, 1\n" \
    "v_mbcnt_lo_u32_b32 v17, -1, 0\n" \
    "v_mbcnt_hi_u32_b32 v17, -1, v17\n" \
    "v_lshlrev_b32 v0, 4, v17\n" \
    "s_getpc_b64 s[0:1]\n" \
    "LSpc_%=:\n" \
    "s_lshl_b32 s2, s14, 12\n" \
    "v_lshl_add_u32 v110, v17, 6, s2\n" \
    "v_min_u32 v110, LSend_%=-LSpc_%=, v110\n" \
    "s_mov_b32 m0, 99328\n" \
    "s_nop 0\n" \
    "global_load_lds_dword v110, s[0:1]\n" \
    "s_add_u32 s26, s13, 0\n" \
    "s_min_u32 s26, s26, 63\n" \
    "s_lshl_b32 s26, s26, 2\n" \
    "s_add_u32 s26, s26, s14\n" \
    "s_lshl_b32 s26, s26, 14\n" \
    "s_lshr_b32 s27, s26, 8\n" \
    "s_and_b32 s27, s27, 0x2000\n" \
    "s_add_u32 s26, s26, s27\n" \
    "s_add_u32 s26, s26, 44040192\n" \
    "s_mul_i32 s27, s14, 7168\n" \
    "s_add_u32 m0, s27, s8\n" \
    "s_nop 0\n" \
    "buffer_load_dwordx4 v0, s[4:7], s26 offen offset:0 lds\n" \
    "buffer_load_dwordx4 v0, s[4:7], s26 offen offset:1024 lds\n" \
    "buffer_load_dwordx4 v0, s[4:7], s26 offen offset:2048 lds\n" \
    "buffer_load_dwordx4 v0, s[4:7], s26 offen offset:3072 lds\n" \
    "s_add_u32 m0, m0, 4096\n" \
    "s_add_u32 s26, s26, 4096\n" \
    "buffer_load_dwordx4 v0, s[4:7], s26 offen offset:0 lds\n" \
    "buffer_load_dwordx4 v0, s[4:7], s26 offen offset:1024 lds\n" \
    "buffer_load_dwordx4 v0, s[4:7], s26 offen offset:2048 lds\n" \
    "s_lshl_b32 s0, s12, 3\n" \
    "s_add_u32 s0, s0, s14\n" \
    "s_lshl_b32 s0, s0, 17\n" \
    "s_add_u32 s0, s0, 27262976\n" \
    "s_lshr_b32 s1, s0, 8\n" \
    "s_and_b32 s1, s1, 0x2000\n" \
    "s_or_b32 s0, s0, s1\n" \
    "s_mov_b32 s1, s20\n" \
    "s_cmp_gt_u32 s1, 6\n" \
    "s_cselect_b32 s3, 517120, 0\n" \
    "s_lshl_b32 s2, s1, 10\n" \
    "s_add_u32 s2, s2, s3\n" \
    "s_add_u32 s2, s2, s0\n" \
    "s_add_u32 s1, s1, 1\n" \
    "s_cmp_eq_u32 s1, 14\n" \
    "s_cselect_b32 s1, 0, s1\n" \
    "buffer_load_dwordx4 a[0:3], v0, s[4:7], s2 offen\n" \
    "s_add_u32 s2, s2, 16384\n" \
    "buffer_load_dwordx4 a[8:11], v0, s[4:7], s2 offen\n" \
    "s_add_u32 s2, s2, 16384\n" \
    "s_xor_b32 s2, s2, 0x2000\n" \
    "buffer_load_dwordx4 a[16:19], v0, s[4:7], s2 offen\n" \
    "s_add_u32 s2, s2, 16384\n" \
    "s_xor_b32 s2, s2, 0x2000\n" \
    "buffer_load_dwordx4 a[24:27], v0, s[4:7], s2 offen\n" \
    "s_add_u32 s2, s2, 16384\n" \
    "buffer_load_dwordx4 a[32:35], v0, s[4:7], s2 offen\n" \
    "s_add_u32 s2, s2, 16384\n" \
    "s_xor_b32 s2, s2, 0x2000\n" \
    "buffer_load_dwordx4 a[40:43], v0, s[4:7], s2 offen\n" \
    "s_add_u32 s2, s2, 16384\n" \
    "s_xor_b32 s2, s2, 0x2000\n" \
    "buffer_load_dwordx4 a[48:51], v0, s[4:7], s2 offen\n" \
    "s_cmp_gt_u32 s1, 6\n" \
    "s_cselect_b32 s3, 517120, 0\n" \
    "s_lshl_b32 s2, s1, 10\n" \
    "s_add_u32 s2, s2, s3\n" \
    "s_add_u32 s2, s2, s0\n" \
    "s_add_u32 s1, s1, 1\n" \
    "s_cmp_eq_u32 s1, 14\n" \
    "s_cselect_b32 s1, 0, s1\n" \
    "buffer_load_dwordx4 a[4:7], v0, s[4:7], s2 offen\n" \
    "s_add_u32 s2, s2, 16384\n" \
    "buffer_load_dwordx4 a[12:15], v0, s[4:7], s2 offen\n" \
    "s_add_u32 s2, s2, 16384\n" \
    "s_xor_b32 s2, s2, 0x2000\n" \
    "buffer_load_dwordx4 a[20:23], v0, s[4:7], s2 offen\n" \
    "s_add_u32 s2, s2, 16384\n" \
    "s_xor_b32 s2, s2, 0x2000\n" \
    "buffer_load_dwordx4 a[28:31], v0, s[4:7], s2 offen\n" \
    "s_add_u32 s2, s2, 16384\n" \
    "buffer_load_dwordx4 a[36:39], v0, s[4:7], s2 offen\n" \
    "s_add_u32 s2, s2, 16384\n" \
    "s_xor_b32 s2, s2, 0x2000\n" \
    "buffer_load_dwordx4 a[44:47], v0, s[4:7], s2 offen\n" \
    "s_add_u32 s2, s2, 16384\n" \
    "s_xor_b32 s2, s2, 0x2000\n" \
    "buffer_load_dwordx4 a[52:55], v0, s[4:7], s2 offen\n" \
    "v_and_b32 v110, 15, v17\n" \
    "v_lshrrev_b32 v111, 4, v17\n" \
    "v_lshlrev_b32 v10, 2, v17\n" \
    "v_and_b32 v112, 1, v17\n" \
    "v_cmp_eq_u32 vcc, 1, v112\n" \
    "v_mov_b32 v112, 0x4444\n" \
    "v_mov_b32 v113, 0xeeee\n" \
    "v_cndmask_b32 v1, v112, v113, vcc\n" \
    "v_lshlrev_b32 v112, 1, v17\n" \
    "v_mov_b32 v113, 0x40000000\n" \
    "v_cndmask_b32 v11, v112, v113, vcc\n" \
    "v_mov_b32 v113, 0x3f803f80\n" \
    "v_lshlrev_b32 v112, 2, v111\n" \
    "v_sub_u32 v112, v110, v112\n" \
    "v_cmp_eq_u32 vcc, 0, v112\n" \
    "s_nop 1\n" \
    "v_cndmask_b32 v116, 0, v113, vcc\n" \
    "v_cmp_eq_u32 vcc, 1, v112\n" \
    "s_nop 1\n" \
    "v_cndmask_b32 v117, 0, v113, vcc\n" \
    "v_cmp_eq_u32 vcc, 2, v112\n" \
    "s_nop 1\n" \
    "v_cndmask_b32 v118, 0, v113, vcc\n" \
    "v_cmp_eq_u32 vcc, 3, v112\n" \
    "s_nop 1\n" \
    "v_cndmask_b32 v119, 0, v113, vcc\n" \
    "s_lshl_b32 s24, s20, 9\n" \
    "s_add_u32 s24, s24, 0\n" \
    "v_add_u32 v4, s24, v0\n" \
    "s_lshl_b32 s24, s14, 10\n" \
    "v_add_u32 v6, s24, v0\n" \
    "v_lshrrev_b32 v112, 3, v17\n" \
    "v_add_u32 v112, s14, v112\n" \
    "v_and_b32 v112, 3, v112\n" \
    "v_lshlrev_b32 v112, 2, v112\n" \
    "v_lshl_add_u32 v112, v110, 4, v112\n" \
    "v_lshl_add_u32 v5, v111, 10, v112\n" \
    "s_lshl_b32 s24, s14, 11\n" \
    "s_add_u32 s24, s24, 7168\n" \
    "v_lshlrev_b32 v112, 5, v110\n" \
    "v_lshl_add_u32 v112, v111, 3, v112\n" \
    "v_and_b32 v113, 8, v17\n" \
    "v_lshlrev_b32 v113, 1, v113\n" \
    "v_xor_b32 v112, v112, v113\n" \
    "v_add_u32 v7, s24, v112\n" \
    "v_lshlrev_b32 v112, 3, v17\n" \
    "v_lshrrev_b32 v113, 1, v17\n" \
    "v_and_b32 v113, 16, v113\n" \
    "v_xor_b32 v112, v112, v113\n" \
    "v_add_u32 v8, s24, v112\n" \
    "v_and_b32 v112, 7, v110\n" \
    "v_add_u32 v113, s14, v112\n" \
    "v_and_b32 v113, 3, v113\n" \
    "v_lshlrev_b32 v113, 2, v113\n" \
    "v_lshl_add_u32 v9, v112, 7, v113\n" \
    "v_add_u32 v9, 15360, v9\n" \
    "v_bfe_i32 v113, v17, 3, 1\n" \
    "v_xor_b32 v113, v113, v111\n" \
    "v_and_b32 v113, 7, v113\n" \
    "v_lshl_add_u32 v9, v113, 4, v9\n" \
    "v_and_b32 v113, 8, v17\n" \
    "v_lshlrev_b32 v12, 11, v111\n" \
    "v_lshl_add_u32 v12, v110, 5, v12\n" \
    "v_add_u32 v12, 8704, v12\n" \
    "v_lshrrev_b32 v112, 1, v111\n" \
    "v_lshlrev_b32 v13, 8, v112\n" \
    "v_and_b32 v112, 1, v111\n" \
    "v_lshl_add_u32 v13, v112, 3, v13\n" \
    "v_and_b32 v112, 7, v110\n" \
    "v_lshl_add_u32 v13, v112, 5, v13\n" \
    "v_add_u32 v13, 0, v13\n" \
    "s_mul_i32 s24, s14, 7\n" \
    "v_lshrrev_b32 v112, 3, v110\n" \
    "v_add3_u32 v112, v112, s24, 5\n" \
    "v_lshrrev_b32 v113, 1, v112\n" \
    "v_lshl_add_u32 v2, v113, 9, v13\n" \
    "v_and_b32 v113, 1, v112\n" \
    "v_lshl_add_u32 v2, v113, 2, v2\n" \
    "v_subrev_u32 v112, 5, v112\n" \
    "v_lshrrev_b32 v113, 1, v112\n" \
    "v_lshl_add_u32 v13, v113, 9, v13\n" \
    "v_and_b32 v113, 1, v112\n" \
    "v_lshl_add_u32 v13, v113, 2, v13\n" \
    "v_lshrrev_b32 v112, 3, v17\n" \
    "v_and_b32 v113, 3, v17\n" \
    "v_lshlrev_b32 v113, 1, v113\n" \
    "v_cmp_eq_u32 vcc, v112, v113\n" \
    "v_add_u32 v113, 1, v113\n" \
    "s_nop 0\n" \
    "v_cndmask_b32 v15, 0, 1.0, vcc\n" \
    "v_cmp_eq_u32 vcc, v112, v113\n" \
    "s_nop 1\n" \
    "v_cndmask_b32 v16, 0, 1.0, vcc\n" \
    "s_lshl_b32 s24, s14, 3\n" \
    "v_add_u32 v112, s24, v112\n" \
    "v_and_b32 v113, 28, v10\n" \
    "v_sub_u32 v112, v112, v113\n" \
    "v_cmp_le_i32 s[92:93], 0, v112\n" \
    "v_cmp_le_i32 s[94:95], 1, v112\n" \
    "v_cmp_le_i32 s[96:97], 2, v112\n" \
    "v_cmp_le_i32 s[98:99], 3, v112\n" \
    "s_and_b32 s24, s14, 1\n" \
    "s_lshl_b32 s24, s24, 7\n" \
    "s_lshr_b32 s25, s14, 1\n" \
    "s_lshl_b32 s25, s25, 10\n" \
    "s_add_u32 s87, s24, s25\n" \
    "s_and_b32 s24, s12, 3\n" \
    "s_lshl_b32 s24, s24, 11\n" \
    "s_add_u32 s87, s87, s24\n" \
    "s_lshr_b32 s24, s12, 2\n" \
    "s_xor_b32 s25, s24, 1\n" \
    "s_lshl_b32 s25, s25, 13\n" \
    "s_add_u32 s87, s87, s25\n" \
    "s_and_b32 s24, s24, 1\n" \
    "s_lshl_b32 s24, s24, 21\n" \
    "s_add_u32 s87, s87, s24\n" \
    "v_mov_b32 v112, 0\n" \
    "ds_write_b32 v10, v112 offset:20480\n" \
    "v_cmp_eq_u32 vcc, 0, v6\n" \
    "v_mov_b32 v112, 0x40000000\n" \
    "s_nop 0\n" \
    "v_cndmask_b32 v3, v112, 0, vcc\n" \
    "s_waitcnt vmcnt(14)\n" \
    "s_barrier\n" \
    "LSturn_%=:\n" \
    "s_mov_b32 s25, s20\n" \
    "s_cmp_lt_u32 s13, 4\n" \
    "s_cselect_b64 vcc, -1, 0\n" \
    "s_lshl_b32 s10, s25, 10\n" \
    "s_add_u32 s10, s10, s8\n" \
    "v_add_u32 v110, s10, v0\n" \
    "s_add_u32 s25, s25, 1\n" \
    "s_cmp_eq_u32 s25, 14\n" \
    "s_cselect_b32 s25, 0, s25\n" \
    "ds_read_b128 v[34:37], v110\n" \
    "ds_read_b128 v[38:41], v110 offset:14336\n" \
    "s_cbranch_vccnz LSva_%=\n" \
    "LSwa_%=:\n" \
    "s_waitcnt lgkmcnt(0)\n" \
    "v_mfma_f32_16x16x32_bf16 v[50:53], a[0:3], v[34:37], 0\n" \
    "s_lshl_b32 s10, s25, 10\n" \
    "s_add_u32 s10, s10, s8\n" \
    "v_mfma_f32_16x16x32_bf16 v[54:57], a[8:11], v[34:37], 0\n" \
    "v_add_u32 v110, s10, v0\n" \
    "s_add_u32 s25, s25, 1\n" \
    "v_mfma_f32_16x16x32_bf16 v[58:61], a[16:19], v[34:37], 0\n" \
    "s_cmp_eq_u32 s25, 14\n" \
    "s_cselect_b32 s25, 0, s25\n" \
    "v_mfma_f32_16x16x32_bf16 v[62:65], a[24:27], v[34:37], 0\n" \
    "ds_read_b128 v[42:45], v110\n" \
    "ds_read_b128 v[46:49], v110 offset:14336\n" \
    "v_mfma_f32_16x16x32_bf16 v[66:69], a[32:35], v[34:37], 0\n" \
    "s_lshr_b32 s24, s13, 3\n" \
    "s_and_b32 s24, s24, 2\n" \
    "v_mfma_f32_16x16x32_bf16 v[70:73], a[40:43], v[34:37], 0\n" \
    "s_lshl_b32 s30, s13, 15\n" \
    "s_xor_b32 s31, s24, s15\n" \
    "v_mfma_f32_16x16x32_bf16 v[74:77], a[48:51], v[34:37], 0\n" \
    "s_lshl_b32 s31, s31, 13\n" \
    "s_add_u32 s31, s31, s30\n" \
    "v_mfma_f32_16x16x32_bf16 v[78:81], a[0:3], v[38:41], 0\n" \
    "s_xor_b32 s35, s24, s16\n" \
    "s_lshl_b32 s35, s35, 13\n" \
    "v_mfma_f32_16x16x32_bf16 v[82:85], a[8:11], v[38:41], 0\n" \
    "s_add_u32 s35, s35, s30\n" \
    "s_add_u32 s35, s35, 2097152\n" \
    "v_mfma_f32_16x16x32_bf16 v[86:89], a[16:19], v[38:41], 0\n" \
    "s_cmp_eq_u32 s13, 0\n" \
    "s_cselect_b32 s21, 24203264, s31\n" \
    "v_mfma_f32_16x16x32_bf16 v[90:93], a[24:27], v[38:41], 0\n" \
    "s_lshl_b32 s24, s20, 9\n" \
    "s_add_u32 s21, s21, s24\n" \
    "v_mfma_f32_16x16x32_bf16 v[94:97], a[32:35], v[38:41], 0\n" \
    "v_mfma_f32_16x16x32_bf16 v[98:101], a[40:43], v[38:41], 0\n" \
    "v_mfma_f32_16x16x32_bf16 v[102:105], a[48:51], v[38:41], 0\n" \
    "s_cbranch_vccnz LSvb_%=\n" \
    "LSwb_%=:\n" \
    "s_waitcnt lgkmcnt(0)\n" \
    "v_mfma_f32_16x16x32_bf16 v[50:53], a[4:7], v[42:45], v[50:53]\n" \
    "s_lshl_b32 s10, s25, 10\n" \
    "s_add_u32 s10, s10, s8\n" \
    "v_mfma_f32_16x16x32_bf16 v[54:57], a[12:15], v[42:45], v[54:57]\n" \
    "v_add_u32 v110, s10, v0\n" \
    "s_add_u32 s25, s25, 1\n" \
    "v_mfma_f32_16x16x32_bf16 v[58:61], a[20:23], v[42:45], v[58:61]\n" \
    "s_cmp_eq_u32 s25, 14\n" \
    "s_cselect_b32 s25, 0, s25\n" \
    "v_mfma_f32_16x16x32_bf16 v[62:65], a[28:31], v[42:45], v[62:65]\n" \
    "ds_read_b128 v[34:37], v110\n" \
    "ds_read_b128 v[38:41], v110 offset:14336\n" \
    "v_mfma_f32_16x16x32_bf16 v[66:69], a[36:39], v[42:45], v[66:69]\n" \
    "s_xor_b32 s28, s31, 0x4000\n" \
    "s_lshl_b32 s24, s12, 8\n" \
    "v_mfma_f32_16x16x32_bf16 v[70:73], a[44:47], v[42:45], v[70:73]\n" \
    "s_add_u32 s22, s28, s24\n" \
    "s_xor_b32 s23, s35, 0x4000\n" \
    "v_mfma_f32_16x16x32_bf16 v[74:77], a[52:55], v[42:45], v[74:77]\n" \
    "s_lshl_b32 s24, s12, 9\n" \
    "s_add_u32 s23, s23, s24\n" \
    "v_mfma_f32_16x16x32_bf16 v[78:81], a[4:7], v[46:49], v[78:81]\n" \
    "s_lshl_b32 s24, s14, 6\n" \
    "s_add_u32 s23, s23, s24\n" \
    "v_mfma_f32_16x16x32_bf16 v[82:85], a[12:15], v[46:49], v[82:85]\n" \
    "v_mov_b32 v14, 0x40000000\n" \
    "s_cmp_lg_u32 s14, 1\n" \
    "v_mfma_f32_16x16x32_bf16 v[86:89], a[20:23], v[46:49], v[86:89]\n" \
    "s_cselect_b32 s30, 0x40000000, 0\n" \
    "s_lshl_b32 s24, s12, 6\n" \
    "v_mfma_f32_16x16x32_bf16 v[90:93], a[28:31], v[46:49], v[90:93]\n" \
    "s_add_u32 s24, s24, 7168\n" \
    "s_add_u32 s24, s24, s30\n" \
    "v_mfma_f32_16x16x32_bf16 v[94:97], a[36:39], v[46:49], v[94:97]\n" \
    "v_mfma_f32_16x16x32_bf16 v[98:101], a[44:47], v[46:49], v[98:101]\n" \
    "v_mfma_f32_16x16x32_bf16 v[102:105], a[52:55], v[46:49], v[102:105]\n" \
    "s_cbranch_vccnz LSvc_%=\n" \
    "LSwc_%=:\n" \
    "s_waitcnt lgkmcnt(0)\n" \
    "v_mfma_f32_16x16x32_bf16 v[50:53], a[56:59], v[34:37], v[50:53]\n" \
    "s_lshl_b32 s10, s25, 10\n" \
    "s_add_u32 s10, s10, s8\n" \
    "v_mfma_f32_16x16x32_bf16 v[54:57], a[64:67], v[34:37], v[54:57]\n" \
    "v_add_u32 v110, s10, v0\n" \
    "s_add_u32 s25, s25, 1\n" \
    "v_mfma_f32_16x16x32_bf16 v[58:61], a[72:75], v[34:37], v[58:61]\n" \
    "s_cmp_eq_u32 s25, 14\n" \
    "s_cselect_b32 s25, 0, s25\n" \
    "v_mfma_f32_16x16x32_bf16 v[62:65], a[80:83], v[34:37], v[62:65]\n" \
    "ds_read_b128 v[42:45], v110\n" \
    "ds_read_b128 v[46:49], v110 offset:14336\n" \
    "v_mfma_f32_16x16x32_bf16 v[66:69], a[88:91], v[34:37], v[66:69]\n" \
    "s_add_u32 s29, s31, s24\n" \
    "v_writelane_b32 v14, s29, 0\n" \
    "v_mfma_f32_16x16x32_bf16 v[70:73], a[96:99], v[34:37], v[70:73]\n" \
    "s_add_u32 s89, s35, s24\n" \
    "s_lshl_b32 s24, s12, 7\n" \
    "v_mfma_f32_16x16x32_bf16 v[74:77], a[104:107], v[34:37], v[74:77]\n" \
    "s_add_u32 s24, s24, s28\n" \
    "s_add_u32 s24, s24, s30\n" \
    "v_mfma_f32_16x16x32_bf16 v[78:81], a[56:59], v[38:41], v[78:81]\n" \
    "s_add_u32 s29, s24, 4096\n" \
    "v_writelane_b32 v14, s29, 1\n" \
    "v_mfma_f32_16x16x32_bf16 v[82:85], a[64:67], v[38:41], v[82:85]\n" \
    "s_add_u32 s29, s24, 6144\n" \
    "v_writelane_b32 v14, s29, 2\n" \
    "v_mfma_f32_16x16x32_bf16 v[86:89], a[72:75], v[38:41], v[86:89]\n" \
    "v_mfma_f32_16x16x32_bf16 v[90:93], a[80:83], v[38:41], v[90:93]\n" \
    "v_mfma_f32_16x16x32_bf16 v[94:97], a[88:91], v[38:41], v[94:97]\n" \
    "v_mfma_f32_16x16x32_bf16 v[98:101], a[96:99], v[38:41], v[98:101]\n" \
    "v_mfma_f32_16x16x32_bf16 v[102:105], a[104:107], v[38:41], v[102:105]\n" \
    "s_cbranch_vccnz LSvd_%=\n" \
    "LSwd_%=:\n" \
    "s_waitcnt lgkmcnt(0)\n" \
    "v_mfma_f32_16x16x32_bf16 v[50:53], a[60:63], v[42:45], v[50:53]\n" \
    "s_lshl_b32 s10, s25, 10\n" \
    "s_add_u32 s10, s10, s8\n" \
    "v_mfma_f32_16x16x32_bf16 v[54:57], a[68:71], v[42:45], v[54:57]\n" \
    "v_add_u32 v110, s10, v0\n" \
    "s_add_u32 s25, s25, 1\n" \
    "v_mfma_f32_16x16x32_bf16 v[58:61], a[76:79], v[42:45], v[58:61]\n" \
    "s_cmp_eq_u32 s25, 14\n" \
    "s_cselect_b32 s25, 0, s25\n" \
    "v_mfma_f32_16x16x32_bf16 v[62:65], a[84:87], v[42:45], v[62:65]\n" \
    "ds_read_b128 v[34:37], v110\n" \
    "ds_read_b128 v[38:41], v110 offset:14336\n" \
    "v_mfma_f32_16x16x32_bf16 v[66:69], a[92:95], v[42:45], v[66:69]\n" \
    "v_mfma_f32_16x16x32_bf16 v[70:73], a[100:103], v[42:45], v[70:73]\n" \
    "v_mfma_f32_16x16x32_bf16 v[74:77], a[108:111], v[42:45], v[74:77]\n" \
    "v_mfma_f32_16x16x32_bf16 v[78:81], a[60:63], v[46:49], v[78:81]\n" \
    "v_mfma_f32_16x16x32_bf16 v[82:85], a[68:71], v[46:49], v[82:85]\n" \
    "v_mfma_f32_16x16x32_bf16 v[86:89], a[76:79], v[46:49], v[86:89]\n" \
    "v_mfma_f32_16x16x32_bf16 v[90:93], a[84:87], v[46:49], v[90:93]\n" \
    "v_mfma_f32_16x16x32_bf16 v[94:97], a[92:95], v[46:49], v[94:97]\n" \
    "v_mfma_f32_16x16x32_bf16 v[98:101], a[100:103], v[46:49], v[98:101]\n" \
    "v_mfma_f32_16x16x32_bf16 v[102:105], a[108:111], v[46:49], v[102:105]\n" \
    "s_cbranch_vccnz LSve_%=\n" \
    "LSwe_%=:\n" \
    "s_waitcnt lgkmcnt(0)\n" \
    "v_mfma_f32_16x16x32_bf16 v[50:53], a[112:115], v[34:37], v[50:53]\n" \
    "s_lshl_b32 s10, s25, 10\n" \
    "s_add_u32 s10, s10, s8\n" \
    "v_mfma_f32_16x16x32_bf16 v[54:57], a[120:123], v[34:37], v[54:57]\n" \
    "v_add_u32 v110, s10, v0\n" \
    "s_add_u32 s25, s25, 1\n" \
    "v_mfma_f32_16x16x32_bf16 v[58:61], a[128:131], v[34:37], v[58:61]\n" \
    "s_cmp_eq_u32 s25, 14\n" \
    "s_cselect_b32 s25, 0, s25\n" \
    "v_mfma_f32_16x16x32_bf16 v[62:65], a[136:139], v[34:37], v[62:65]\n" \
    "ds_read_b128 v[42:45], v110\n" \
    "ds_read_b128 v[46:49], v110 offset:14336\n" \
    "v_mfma_f32_16x16x32_bf16 v[66:69], a[144:147], v[34:37], v[66:69]\n" \
    "v_mfma_f32_16x16x32_bf16 v[70:73], a[152:155], v[34:37], v[70:73]\n" \
    "v_mfma_f32_16x16x32_bf16 v[74:77], a[160:163], v[34:37], v[74:77]\n" \
    "s_cmp_lt_u32 s13, 4\n" \
    "s_cbranch_scc1 LSnoflag_%=\n" \
    "s_waitcnt vmcnt(0)\n" \
    "s_barrier\n" \
    "buffer_store_dword v1, v3, s[4:7], s11 offen sc1\n" \
    "LSnoflag_%=:\n" \
    "v_mfma_f32_16x16x32_bf16 v[78:81], a[112:115], v[38:41], v[78:81]\n" \
    "s_add_u32 s26, s13, 4\n" \
    "s_min_u32 s26, s26, 63\n" \
    "v_mfma_f32_16x16x32_bf16 v[82:85], a[120:123], v[38:41], v[82:85]\n" \
    "s_lshl_b32 s26, s26, 2\n" \
    "s_add_u32 s26, s26, s14\n" \
    "v_mfma_f32_16x16x32_bf16 v[86:89], a[128:131], v[38:41], v[86:89]\n" \
    "s_lshl_b32 s26, s26, 14\n" \
    "s_lshr_b32 s27, s26, 8\n" \
    "v_mfma_f32_16x16x32_bf16 v[90:93], a[136:139], v[38:41], v[90:93]\n" \
    "s_and_b32 s27, s27, 0x2000\n" \
    "s_add_u32 s26, s26, s27\n" \
    "v_mfma_f32_16x16x32_bf16 v[94:97], a[144:147], v[38:41], v[94:97]\n" \
    "s_add_u32 s26, s26, 44040192\n" \
    "s_mul_i32 s27, s14, 7168\n" \
    "v_mfma_f32_16x16x32_bf16 v[98:101], a[152:155], v[38:41], v[98:101]\n" \
    "s_add_u32 m0, s27, s9\n" \
    "s_nop 0\n" \
    "v_mfma_f32_16x16x32_bf16 v[102:105], a[160:163], v[38:41], v[102:105]\n" \
    "buffer_load_dwordx4 v0, s[4:7], s26 offen offset:0 lds\n" \
    "s_cbranch_vccnz LSvf_%=\n" \
    "LSwf_%=:\n" \
    "s_waitcnt lgkmcnt(0)\n" \
    "v_mfma_f32_16x16x32_bf16 v[50:53], a[116:119], v[42:45], v[50:53]\n" \
    "s_lshl_b32 s10, s25, 10\n" \
    "s_add_u32 s10, s10, s8\n" \
    "v_mfma_f32_16x16x32_bf16 v[54:57], a[124:127], v[42:45], v[54:57]\n" \
    "v_add_u32 v110, s10, v0\n" \
    "s_add_u32 s25, s25, 1\n" \
    "v_mfma_f32_16x16x32_bf16 v[58:61], a[132:135], v[42:45], v[58:61]\n" \
    "s_cmp_eq_u32 s25, 14\n" \
    "s_cselect_b32 s25, 0, s25\n" \
    "v_mfma_f32_16x16x32_bf16 v[62:65], a[140:143], v[42:45], v[62:65]\n" \
    "ds_read_b128 v[34:37], v110\n" \
    "ds_read_b128 v[38:41], v110 offset:14336\n" \
    "v_mfma_f32_16x16x32_bf16 v[66:69], a[148:151], v[42:45], v[66:69]\n" \
    "v_mfma_f32_16x16x32_bf16 v[70:73], a[156:159], v[42:45], v[70:73]\n" \
    "v_mfma_f32_16x16x32_bf16 v[74:77], a[164:167], v[42:45], v[74:77]\n" \
    "v_mfma_f32_16x16x32_bf16 v[78:81], a[116:119], v[46:49], v[78:81]\n" \
    "v_mfma_f32_16x16x32_bf16 v[82:85], a[124:127], v[46:49], v[82:85]\n" \
    "buffer_load_dwordx4 v0, s[4:7], s26 offen offset:1024 lds\n" \
    "buffer_load_dwordx4 v0, s[4:7], s26 offen offset:2048 lds\n" \
    "v_mfma_f32_16x16x32_bf16 v[86:89], a[132:135], v[46:49], v[86:89]\n" \
    "buffer_load_dwordx4 v0, s[4:7], s26 offen offset:3072 lds\n" \
    "v_mfma_f32_16x16x32_bf16 v[90:93], a[140:143], v[46:49], v[90:93]\n" \
    "v_mfma_f32_16x16x32_bf16 v[94:97], a[148:151], v[46:49], v[94:97]\n" \
    "v_mfma_f32_16x16x32_bf16 v[98:101], a[156:159], v[46:49], v[98:101]\n" \
    "v_mfma_f32_16x16x32_bf16 v[102:105], a[164:167], v[46:49], v[102:105]\n" \
    "s_cbranch_vccnz LSvg_%=\n" \
    "LSwg_%=:\n" \
    "s_waitcnt lgkmcnt(0)\n" \
    "v_mfma_f32_16x16x32_bf16 v[50:53], a[168:171], v[34:37], v[50:53]\n" \
    "s_lshl_b32 s10, s25, 10\n" \
    "s_add_u32 s10, s10, s8\n" \
    "v_mfma_f32_16x16x32_bf16 v[54:57], a[176:179], v[34:37], v[54:57]\n" \
    "v_add_u32 v110, s10, v0\n" \
    "s_add_u32 s25, s25, 1\n" \
    "v_mfma_f32_16x16x32_bf16 v[58:61], a[184:187], v[34:37], v[58:61]\n" \
    "s_cmp_eq_u32 s25, 14\n" \
    "s_cselect_b32 s25, 0, s25\n" \
    "v_mfma_f32_16x16x32_bf16 v[62:65], a[192:195], v[34:37], v[62:65]\n" \
    "ds_read_b128 v[42:45], v110\n" \
    "ds_read_b128 v[46:49], v110 offset:14336\n" \
    "v_mfma_f32_16x16x32_bf16 v[66:69], a[200:203], v[34:37], v[66:69]\n" \
    "v_mfma_f32_16x16x32_bf16 v[70:73], a[208:211], v[34:37], v[70:73]\n" \
    "v_mfma_f32_16x16x32_bf16 v[74:77], a[216:219], v[34:37], v[74:77]\n" \
    "v_mfma_f32_16x16x32_bf16 v[78:81], a[168:171], v[38:41], v[78:81]\n" \
    "v_mfma_f32_16x16x32_bf16 v[82:85], a[176:179], v[38:41], v[82:85]\n" \
    "s_add_u32 m0, m0, 4096\n" \
    "s_add_u32 s26, s26, 4096\n" \
    "buffer_load_dwordx4 v0, s[4:7], s26 offen offset:0 lds\n" \
    "v_mfma_f32_16x16x32_bf16 v[86:89], a[184:187], v[38:41], v[86:89]\n" \
    "buffer_load_dwordx4 v0, s[4:7], s26 offen offset:1024 lds\n" \
    "buffer_load_dwordx4 v0, s[4:7], s26 offen offset:2048 lds\n" \
    "v_mfma_f32_16x16x32_bf16 v[90:93], a[192:195], v[38:41], v[90:93]\n" \
    "v_mfma_f32_16x16x32_bf16 v[94:97], a[200:203], v[38:41], v[94:97]\n" \
    "v_mfma_f32_16x16x32_bf16 v[98:101], a[208:211], v[38:41], v[98:101]\n" \
    "v_mfma_f32_16x16x32_bf16 v[102:105], a[216:219], v[38:41], v[102:105]\n" \
    "s_cbranch_vccnz LSvh_%=\n" \
    "LSwh_%=:\n" \
    "s_waitcnt lgkmcnt(0)\n" \
    "v_mfma_f32_16x16x32_bf16 v[50:53], a[172:175], v[42:45], v[50:53]\n" \
    "s_lshl_b32 s10, s25, 10\n" \
    "s_add_u32 s10, s10, s8\n" \
    "v_mfma_f32_16x16x32_bf16 v[54:57], a[180:183], v[42:45], v[54:57]\n" \
    "v_add_u32 v110, s10, v0\n" \
    "s_add_u32 s25, s25, 1\n" \
    "v_mfma_f32_16x16x32_bf16 v[58:61], a[188:191], v[42:45], v[58:61]\n" \
    "s_cmp_eq_u32 s25, 14\n" \
    "s_cselect_b32 s25, 0, s25\n" \
    "v_mfma_f32_16x16x32_bf16 v[62:65], a[196:199], v[42:45], v[62:65]\n" \
    "ds_read_b128 v[34:37], v110\n" \
    "ds_read_b128 v[38:41], v110 offset:14336\n" \
    "v_mfma_f32_16x16x32_bf16 v[66:69], a[204:207], v[42:45], v[66:69]\n" \
    "v_lshrrev_b32 v113, 4, v17\n" \
    "v_xor_b32 v113, v113, v17\n" \
    "v_mfma_f32_16x16x32_bf16 v[70:73], a[212:215], v[42:45], v[70:73]\n" \
    "v_and_b32 v113, 7, v113\n" \
    "v_and_b32 v111, 15, v17\n" \
    "v_mfma_f32_16x16x32_bf16 v[74:77], a[220:223], v[42:45], v[74:77]\n" \
    "v_lshl_add_u32 v111, v111, 3, v113\n" \
    "s_lshl_b32 s24, s14, 12\n" \
    "v_mfma_f32_16x16x32_bf16 v[78:81], a[172:175], v[46:49], v[78:81]\n" \
    "v_lshl_add_u32 v111, v111, 4, s24\n" \
    "v_xor_b32 v112, 64, v111\n" \
    "v_mfma_f32_16x16x32_bf16 v[82:85], a[180:183], v[46:49], v[82:85]\n" \
    "v_add_u32 v107, 1024, v13\n" \
    "v_mov_b32 v113, 20480\n" \
    "v_mfma_f32_16x16x32_bf16 v[86:89], a[188:191], v[46:49], v[86:89]\n" \
    "s_mov_b32 s34, 0xff00ff00\n" \
    "s_mov_b32 s35, s34\n" \
    "v_mfma_f32_16x16x32_bf16 v[90:93], a[196:199], v[46:49], v[90:93]\n" \
    "v_cndmask_b32 v107, v107, v113, s[34:35]\n" \
    "v_and_b32 v113, 16, v17\n" \
    "v_mfma_f32_16x16x32_bf16 v[94:97], a[204:207], v[46:49], v[94:97]\n" \
    "v_lshlrev_b32 v113, 4, v113\n" \
    "v_and_b32 v109, 0x4000001e, v11\n" \
    "v_mfma_f32_16x16x32_bf16 v[98:101], a[212:215], v[46:49], v[98:101]\n" \
    "v_or_b32 v109, v109, v113\n" \
    "v_and_b32 v113, 32, v17\n" \
    "v_mfma_f32_16x16x32_bf16 v[102:105], a[220:223], v[46:49], v[102:105]\n" \
    "v_or_b32 v109, v109, v113\n" \
    "s_cbranch_vccnz LSvi_%=\n" \
    "LSwi_%=:\n" \
    "s_waitcnt lgkmcnt(0)\n" \
    "v_mfma_f32_16x16x32_bf16 v[50:53], a[224:227], v[34:37], v[50:53]\n" \
    "s_lshl_b32 s10, s25, 10\n" \
    "s_add_u32 s10, s10, s8\n" \
    "v_mfma_f32_16x16x32_bf16 v[54:57], a[232:235], v[34:37], v[54:57]\n" \
    "v_add_u32 v110, s10, v0\n" \
    "s_add_u32 s25, s25, 1\n" \
    "v_mfma_f32_16x16x32_bf16 v[58:61], a[240:243], v[34:37], v[58:61]\n" \
    "s_cmp_eq_u32 s25, 14\n" \
    "s_cselect_b32 s25, 0, s25\n" \
    "v_mfma_f32_16x16x32_bf16 v[62:65], a[248:251], v[34:37], v[62:65]\n" \
    "ds_read_b128 v[42:45], v110\n" \
    "ds_read_b128 v[46:49], v110 offset:14336\n" \
    "v_mfma_f32_16x16x32_bf16 v[66:69], v[120:123], v[34:37], v[66:69]\n" \
    "v_mov_b32 v18, 0x7fc0dead\n" \
    "v_mov_b32 v19, 0x7fc0dead\n" \
    "v_mfma_f32_16x16x32_bf16 v[70:73], v[128:131], v[34:37], v[70:73]\n" \
    "v_mov_b32 v20, 0x7fc0dead\n" \
    "v_mov_b32 v21, 0x7fc0dead\n" \
    "v_mfma_f32_16x16x32_bf16 v[74:77], v[136:139], v[34:37], v[74:77]\n" \
    "v_mov_b32 v22, 0\n" \
    "ds_write_b32 v9, v18\n" \
    "v_mfma_f32_16x16x32_bf16 v[78:81], a[224:227], v[38:41], v[78:81]\n" \
    "ds_write_b32 v8, v22 offset:1536\n" \
    "ds_write_b128 v6, v[18:21] offset:16384\n" \
    "v_mfma_f32_16x16x32_bf16 v[82:85], a[232:235], v[38:41], v[82:85]\n" \
    "v_mfma_f32_16x16x32_bf16 v[86:89], a[240:243], v[38:41], v[86:89]\n" \
    "v_mfma_f32_16x16x32_bf16 v[90:93], a[248:251], v[38:41], v[90:93]\n" \
    "v_mfma_f32_16x16x32_bf16 v[94:97], v[120:123], v[38:41], v[94:97]\n" \
    "v_mfma_f32_16x16x32_bf16 v[98:101], v[128:131], v[38:41], v[98:101]\n" \
    "v_mfma_f32_16x16x32_bf16 v[102:105], v[136:139], v[38:41], v[102:105]\n" \
    "s_cmp_lt_u32 s13, 4\n" \
    "s_cbranch_scc1 LSnopause_%=\n" \
    "s_cmp_lg_u32 s14, 1\n" \
    "s_cbranch_scc1 LSresume_%=\n" \
    "s_lshl_b32 s34, s12, 6\n" \
    "s_add_u32 s34, s34, 7168\n" \
    "v_mov_b32 v114, s34\n" \
    "s_sub_u32 s34, s13, 3\n" \
    "s_lshr_b32 s24, s34, 3\n" \
    "s_and_b32 s24, s24, 2\n" \
    "s_xor_b32 s24, s24, s15\n" \
    "s_lshl_b32 s24, s24, 13\n" \
    "s_lshl_b32 s34, s34, 15\n" \
    "s_add_u32 s24, s24, s34\n" \
    "LSpz3_%=:\n" \
    "buffer_load_dword v115, v114, s[4:7], s24 offen sc1\n" \
    "s_sub_u32 s17, s17, 1\n" \
    "s_cmp_lt_i32 s17, 0\n" \
    "s_waitcnt vmcnt(0)\n" \
    "s_cbranch_scc1 LSresume_%=\n" \
    "v_readfirstlane_b32 s88, v115\n" \
    "s_nop 0\n" \
    "s_cmp_eq_u32 s88, -1\n" \
    "s_cbranch_scc1 LSpz3_%=\n" \
    "s_add_u32 s88, s88, s66\n" \
    "LSpb3_%=:\n" \
    "s_memrealtime s[34:35]\n" \
    "s_sub_u32 s17, s17, 1\n" \
    "s_waitcnt lgkmcnt(0)\n" \
    "s_mul_i32 s34, s34, 24\n" \
    "s_sub_u32 s34, s88, s34\n" \
    "s_min_i32 s34, s34, s17\n" \
    "s_cmp_gt_i32 s34, 0\n" \
    "s_cbranch_scc1 LSpb3_%=\n" \
    "s_sub_u32 s34, s13, 2\n" \
    "s_lshr_b32 s24, s34, 3\n" \
    "s_and_b32 s24, s24, 2\n" \
    "s_xor_b32 s24, s24, s15\n" \
    "s_lshl_b32 s24, s24, 13\n" \
    "s_lshl_b32 s34, s34, 15\n" \
    "s_add_u32 s24, s24, s34\n" \
    "LSpz2_%=:\n" \
    "buffer_load_dword v115, v114, s[4:7], s24 offen sc1\n" \
    "s_sub_u32 s17, s17, 1\n" \
    "s_cmp_lt_i32 s17, 0\n" \
    "s_waitcnt vmcnt(0)\n" \
    "s_cbranch_scc1 LSresume_%=\n" \
    "v_readfirstlane_b32 s88, v115\n" \
    "s_nop 0\n" \
    "s_cmp_eq_u32 s88, -1\n" \
    "s_cbranch_scc1 LSpz2_%=\n" \
    "s_add_u32 s88, s88, s67\n" \
    "LSpb2_%=:\n" \
    "s_memrealtime s[34:35]\n" \
    "s_sub_u32 s17, s17, 1\n" \
    "s_waitcnt lgkmcnt(0)\n" \
    "s_mul_i32 s34, s34, 24\n" \
    "s_sub_u32 s34, s88, s34\n" \
    "s_min_i32 s34, s34, s17\n" \
    "s_cmp_gt_i32 s34, 0\n" \
    "s_cbranch_scc1 LSpb2_%=\n" \
    "LSresume_%=:\n" \
    "s_barrier\n" \
    "LSnopause_%=:\n" \
    "s_cbranch_vccnz LSvj_%=\n" \
    "LSwj_%=:\n" \
    "s_waitcnt lgkmcnt(0)\n" \
    "v_mfma_f32_16x16x32_bf16 v[50:53], a[228:231], v[42:45], v[50:53]\n" \
    "s_lshl_b32 s10, s25, 10\n" \
    "s_add_u32 s10, s10, s8\n" \
    "v_mfma_f32_16x16x32_bf16 v[54:57], a[236:239], v[42:45], v[54:57]\n" \
    "v_add_u32 v110, s10, v0\n" \
    "s_add_u32 s25, s25, 1\n" \
    "v_mfma_f32_16x16x32_bf16 v[58:61], a[244:247], v[42:45], v[58:61]\n" \
    "s_cmp_eq_u32 s25, 14\n" \
    "s_cselect_b32 s25, 0, s25\n" \
    "v_mfma_f32_16x16x32_bf16 v[62:65], a[252:255], v[42:45], v[62:65]\n" \
    "ds_read_b128 v[34:37], v110\n" \
    "ds_read_b128 v[38:41], v110 offset:14336\n" \
    "v_mfma_f32_16x16x32_bf16 v[66:69], v[124:127], v[42:45], v[66:69]\n" \
    "v_mfma_f32_16x16x32_bf16 v[70:73], v[132:135], v[42:45], v[70:73]\n" \
    "v_mfma_f32_16x16x32_bf16 v[74:77], v[140:143], v[42:45], v[74:77]\n" \
    "v_mfma_f32_16x16x32_bf16 v[78:81], a[228:231], v[46:49], v[78:81]\n" \
    "v_mfma_f32_16x16x32_bf16 v[82:85], a[236:239], v[46:49], v[82:85]\n" \
    "v_mfma_f32_16x16x32_bf16 v[86:89], a[244:247], v[46:49], v[86:89]\n" \
    "v_mfma_f32_16x16x32_bf16 v[90:93], a[252:255], v[46:49], v[90:93]\n" \
    "v_mfma_f32_16x16x32_bf16 v[94:97], v[124:127], v[46:49], v[94:97]\n" \
    "v_mfma_f32_16x16x32_bf16 v[98:101], v[132:135], v[46:49], v[98:101]\n" \
    "v_mfma_f32_16x16x32_bf16 v[102:105], v[140:143], v[46:49], v[102:105]\n" \
    "s_cbranch_vccnz LSvk_%=\n" \
    "LSwk_%=:\n" \
    "s_waitcnt lgkmcnt(0)\n" \
    "v_mfma_f32_16x16x32_bf16 v[50:53], v[144:147], v[34:37], v[50:53]\n" \
    "s_lshl_b32 s10, s25, 10\n" \
    "s_add_u32 s10, s10, s8\n" \
    "v_mfma_f32_16x16x32_bf16 v[54:57], v[152:155], v[34:37], v[54:57]\n" \
    "v_add_u32 v110, s10, v0\n" \
    "s_add_u32 s25, s25, 1\n" \
    "v_mfma_f32_16x16x32_bf16 v[58:61], v[160:163], v[34:37], v[58:61]\n" \
    "s_cmp_eq_u32 s25, 14\n" \
    "s_cselect_b32 s25, 0, s25\n" \
    "v_mfma_f32_16x16x32_bf16 v[62:65], v[168:171], v[34:37], v[62:65]\n" \
    "ds_read_b128 v[42:45], v110\n" \
    "ds_read_b128 v[46:49], v110 offset:14336\n" \
    "v_mfma_f32_16x16x32_bf16 v[66:69], v[176:179], v[34:37], v[66:69]\n" \
    "s_mul_i32 s90, s19, s86\n" \
    "s_ashr_i32 s90, s90, 4\n" \
    "v_mfma_f32_16x16x32_bf16 v[70:73], v[184:187], v[34:37], v[70:73]\n" \
    "s_add_u32 s90, s90, s81\n" \
    "s_and_b32 s90, s90, -8\n" \
    "v_mfma_f32_16x16x32_bf16 v[74:77], v[192:195], v[34:37], v[74:77]\n" \
    "s_cmp_lt_u32 s13, 2\n" \
    "s_cselect_b32 s34, 440, 0\n" \
    "v_mfma_f32_16x16x32_bf16 v[78:81], v[144:147], v[38:41], v[78:81]\n" \
    "s_add_u32 s90, s90, s34\n" \
    "s_ashr_i32 s91, s19, 2\n" \
    "v_mfma_f32_16x16x32_bf16 v[82:85], v[152:155], v[38:41], v[82:85]\n" \
    "s_and_b32 s91, s91, -8\n" \
    "v_mov_b32 v108, 0\n" \
    "v_mfma_f32_16x16x32_bf16 v[86:89], v[160:163], v[38:41], v[86:89]\n" \
    "v_mfma_f32_16x16x32_bf16 v[90:93], v[168:171], v[38:41], v[90:93]\n" \
    "v_mfma_f32_16x16x32_bf16 v[94:97], v[176:179], v[38:41], v[94:97]\n" \
    "v_mfma_f32_16x16x32_bf16 v[98:101], v[184:187], v[38:41], v[98:101]\n" \
    "v_mfma_f32_16x16x32_bf16 v[102:105], v[192:195], v[38:41], v[102:105]\n" \
    "s_cbranch_vccnz LSvl_%=\n" \
    "LSwl_%=:\n" \
    "s_waitcnt lgkmcnt(0)\n" \
    "v_mfma_f32_16x16x32_bf16 v[50:53], v[148:151], v[42:45], v[50:53]\n" \
    "s_lshl_b32 s10, s25, 10\n" \
    "s_add_u32 s10, s10, s8\n" \
    "v_mfma_f32_16x16x32_bf16 v[54:57], v[156:159], v[42:45], v[54:57]\n" \
    "v_add_u32 v110, s10, v0\n" \
    "s_add_u32 s25, s25, 1\n" \
    "v_mfma_f32_16x16x32_bf16 v[58:61], v[164:167], v[42:45], v[58:61]\n" \
    "s_cmp_eq_u32 s25, 14\n" \
    "s_cselect_b32 s25, 0, s25\n" \
    "v_mfma_f32_16x16x32_bf16 v[62:65], v[172:175], v[42:45], v[62:65]\n" \
    "ds_read_b128 v[34:37], v110\n" \
    "ds_read_b128 v[38:41], v110 offset:14336\n" \
    "v_mfma_f32_16x16x32_bf16 v[66:69], v[180:183], v[42:45], v[66:69]\n" \
    "s_sub_u32 s34, s13, 1\n" \
    "s_max_i32 s34, s34, 0\n" \
    "v_mfma_f32_16x16x32_bf16 v[70:73], v[188:191], v[42:45], v[70:73]\n" \
    "s_lshr_b32 s24, s34, 3\n" \
    "s_and_b32 s24, s24, 2\n" \
    "v_mfma_f32_16x16x32_bf16 v[74:77], v[196:199], v[42:45], v[74:77]\n" \
    "s_xor_b32 s24, s24, s15\n" \
    "s_lshl_b32 s24, s24, 13\n" \
    "v_mfma_f32_16x16x32_bf16 v[78:81], v[148:151], v[46:49], v[78:81]\n" \
    "s_lshl_b32 s34, s34, 15\n" \
    "s_add_u32 s24, s24, s34\n" \
    "v_mfma_f32_16x16x32_bf16 v[82:85], v[156:159], v[46:49], v[82:85]\n" \
    "s_add_u32 s31, s24, 7168\n" \
    "v_lshrrev_b32 v115, 4, v17\n" \
    "v_mfma_f32_16x16x32_bf16 v[86:89], v[164:167], v[46:49], v[86:89]\n" \
    "v_lshlrev_b32 v114, 6, v17\n" \
    "v_lshl_add_u32 v114, v115, 28, v114\n" \
    "v_mfma_f32_16x16x32_bf16 v[90:93], v[172:175], v[46:49], v[90:93]\n" \
    "v_or_b32 v114, s30, v114\n" \
    "v_mfma_f32_16x16x32_bf16 v[94:97], v[180:183], v[46:49], v[94:97]\n" \
    "v_mfma_f32_16x16x32_bf16 v[98:101], v[188:191], v[46:49], v[98:101]\n" \
    "v_mfma_f32_16x16x32_bf16 v[102:105], v[196:199], v[46:49], v[102:105]\n" \
    "s_cbranch_vccnz LSvm_%=\n" \
    "LSwm_%=:\n" \
    "s_waitcnt lgkmcnt(0)\n" \
    "v_mfma_f32_16x16x32_bf16 v[50:53], v[200:203], v[34:37], v[50:53]\n" \
    "s_lshl_b32 s10, s25, 10\n" \
    "s_add_u32 s10, s10, s8\n" \
    "v_mfma_f32_16x16x32_bf16 v[54:57], v[208:211], v[34:37], v[54:57]\n" \
    "v_add_u32 v110, s10, v0\n" \
    "s_add_u32 s25, s25, 1\n" \
    "v_mfma_f32_16x16x32_bf16 v[58:61], v[216:219], v[34:37], v[58:61]\n" \
    "s_cmp_eq_u32 s25, 14\n" \
    "s_cselect_b32 s25, 0, s25\n" \
    "v_mfma_f32_16x16x32_bf16 v[62:65], v[224:227], v[34:37], v[62:65]\n" \
    "ds_read_b128 v[42:45], v110\n" \
    "ds_read_b128 v[46:49], v110 offset:14336\n" \
    "v_mfma_f32_16x16x32_bf16 v[66:69], v[232:235], v[34:37], v[66:69]\n" \
    "s_lshl_b32 s2, s12, 1\n" \
    "s_lshr_b32 s0, s14, 1\n" \
    "v_mfma_f32_16x16x32_bf16 v[70:73], v[240:243], v[34:37], v[70:73]\n" \
    "s_add_u32 s2, s2, s0\n" \
    "s_lshl_b32 s2, s2, 3\n" \
    "v_mfma_f32_16x16x32_bf16 v[74:77], v[248:251], v[34:37], v[74:77]\n" \
    "s_and_b32 s0, s14, 1\n" \
    "s_add_u32 s2, s2, s0\n" \
    "v_mfma_f32_16x16x32_bf16 v[78:81], v[200:203], v[38:41], v[78:81]\n" \
    "s_lshl_b32 s2, s2, 5\n" \
    "s_add_u32 s2, s2, 18874368\n" \
    "v_mfma_f32_16x16x32_bf16 v[82:85], v[208:211], v[38:41], v[82:85]\n" \
    "s_lshl_b32 s0, s20, 10\n" \
    "s_add_u32 s0, s0, s8\n" \
    "v_mfma_f32_16x16x32_bf16 v[86:89], v[216:219], v[38:41], v[86:89]\n" \
    "s_cmp_eq_u32 s13, 63\n" \
    "s_addc_u32 s1, s14, 0\n" \
    "v_mfma_f32_16x16x32_bf16 v[90:93], v[224:227], v[38:41], v[90:93]\n" \
    "s_mov_b32 s28, s85\n" \
    "v_add_u32 v110, s0, v0\n" \
    "v_mfma_f32_16x16x32_bf16 v[94:97], v[232:235], v[38:41], v[94:97]\n" \
    "v_mfma_f32_16x16x32_bf16 v[98:101], v[240:243], v[38:41], v[98:101]\n" \
    "v_mfma_f32_16x16x32_bf16 v[102:105], v[248:251], v[38:41], v[102:105]\n" \
    "s_cbranch_vccnz LSvn_%=\n" \
    "LSwn_%=:\n" \
    "s_waitcnt lgkmcnt(0)\n" \
    "v_mfma_f32_16x16x32_bf16 v[50:53], v[204:207], v[42:45], v[50:53]\n" \
    "v_mfma_f32_16x16x32_bf16 v[54:57], v[212:215], v[42:45], v[54:57]\n" \
    "v_mfma_f32_16x16x32_bf16 v[58:61], v[220:223], v[42:45], v[58:61]\n" \
    "v_mfma_f32_16x16x32_bf16 v[62:65], v[228:231], v[42:45], v[62:65]\n" \
    "v_mfma_f32_16x16x32_bf16 v[66:69], v[236:239], v[42:45], v[66:69]\n" \
    "v_mfma_f32_16x16x32_bf16 v[70:73], v[244:247], v[42:45], v[70:73]\n" \
    "v_mfma_f32_16x16x32_bf16 v[74:77], v[252:255], v[42:45], v[74:77]\n" \
    "s_bitcmp1_b32 s14, 0\n" \
    "s_cbranch_scc1 LSodd_%=\n" \
    "v_mfma_f32_16x16x32_bf16 v[78:81], v[204:207], v[46:49], v[78:81]\n" \
    "v_cvt_pk_bf16_f32 v18, v50, v51\n" \
    "v_cvt_pk_bf16_f32 v19, v52, v53\n" \
    "v_mfma_f32_16x16x32_bf16 v[82:85], v[212:215], v[46:49], v[82:85]\n" \
    "v_cvt_pk_bf16_f32 v20, v54, v55\n" \
    "v_cvt_pk_bf16_f32 v21, v56, v57\n" \
    "v_mfma_f32_16x16x32_bf16 v[86:89], v[220:223], v[46:49], v[86:89]\n" \
    "v_cvt_pk_bf16_f32 v22, v58, v59\n" \
    "v_cvt_pk_bf16_f32 v23, v60, v61\n" \
    "v_mfma_f32_16x16x32_bf16 v[90:93], v[228:231], v[46:49], v[90:93]\n" \
    "v_cvt_pk_bf16_f32 v24, v62, v63\n" \
    "v_cvt_pk_bf16_f32 v25, v64, v65\n" \
    "v_mfma_f32_16x16x32_bf16 v[94:97], v[236:239], v[46:49], v[94:97]\n" \
    "v_cvt_pk_bf16_f32 v26, v66, v67\n" \
    "v_cvt_pk_bf16_f32 v27, v68, v69\n" \
    "v_mfma_f32_16x16x32_bf16 v[98:101], v[244:247], v[46:49], v[98:101]\n" \
    "v_cvt_pk_bf16_f32 v28, v70, v71\n" \
    "v_cvt_pk_bf16_f32 v29, v72, v73\n" \
    "v_mfma_f32_16x16x32_bf16 v[102:105], v[252:255], v[46:49], v[102:105]\n" \
    "v_cvt_pk_bf16_f32 v30, v74, v75\n" \
    "v_cvt_pk_bf16_f32 v31, v76, v77\n" \
    "v_mov_b64 v[32:33], 0\n" \
    "ds_read_b128 v[50:53], v110 offset:0\n" \
    "ds_read_b128 v[54:57], v110 offset:1024\n" \
    "ds_read_b128 v[58:61], v110 offset:2048\n" \
    "ds_read_b128 v[62:65], v110 offset:3072\n" \
    "ds_read_b128 v[66:69], v110 offset:14336\n" \
    "ds_read_b128 v[70:73], v110 offset:15360\n" \
    "ds_read_b128 v[74:77], v110 offset:16384\n" \
    "v_cvt_pk_bf16_f32 v34, v78, v79\n" \
    "v_cvt_pk_bf16_f32 v35, v80, v81\n" \
    "ds_read_b128 v[78:81], v110 offset:17408\n" \
    "v_cvt_pk_bf16_f32 v36, v82, v83\n" \
    "v_cvt_pk_bf16_f32 v37, v84, v85\n" \
    "v_cvt_pk_bf16_f32 v38, v86, v87\n" \
    "v_cvt_pk_bf16_f32 v39, v88, v89\n" \
    "v_cvt_pk_bf16_f32 v40, v90, v91\n" \
    "v_cvt_pk_bf16_f32 v41, v92, v93\n" \
    "v_cvt_pk_bf16_f32 v42, v94, v95\n" \
    "v_cvt_pk_bf16_f32 v43, v96, v97\n" \
    "v_cvt_pk_bf16_f32 v44, v98, v99\n" \
    "v_cvt_pk_bf16_f32 v45, v100, v101\n" \
    "v_cvt_pk_bf16_f32 v46, v102, v103\n" \
    "v_cvt_pk_bf16_f32 v47, v104, v105\n" \
    "v_mov_b64 v[48:49], 0\n" \
    "s_branch LSpacked_%=\n" \
    "LSodd_%=:\n" \
    "s_nop 0\n" \
    "v_mfma_f32_16x16x32_bf16 v[78:81], v[204:207], v[46:49], v[78:81]\n" \
    "v_cvt_pk_bf16_f32 v20, v50, v51\n" \
    "v_cvt_pk_bf16_f32 v21, v52, v53\n" \
    "v_mfma_f32_16x16x32_bf16 v[82:85], v[212:215], v[46:49], v[82:85]\n" \
    "v_cvt_pk_bf16_f32 v22, v54, v55\n" \
    "v_cvt_pk_bf16_f32 v23, v56, v57\n" \
    "v_mfma_f32_16x16x32_bf16 v[86:89], v[220:223], v[46:49], v[86:89]\n" \
    "v_cvt_pk_bf16_f32 v24, v58, v59\n" \
    "v_cvt_pk_bf16_f32 v25, v60, v61\n" \
    "v_mfma_f32_16x16x32_bf16 v[90:93], v[228:231], v[46:49], v[90:93]\n" \
    "v_cvt_pk_bf16_f32 v26, v62, v63\n" \
    "v_cvt_pk_bf16_f32 v27, v64, v65\n" \
    "v_mfma_f32_16x16x32_bf16 v[94:97], v[236:239], v[46:49], v[94:97]\n" \
    "v_cvt_pk_bf16_f32 v28, v66, v67\n" \
    "v_cvt_pk_bf16_f32 v29, v68, v69\n" \
    "v_mfma_f32_16x16x32_bf16 v[98:101], v[244:247], v[46:49], v[98:101]\n" \
    "v_cvt_pk_bf16_f32 v30, v70, v71\n" \
    "v_cvt_pk_bf16_f32 v31, v72, v73\n" \
    "v_mfma_f32_16x16x32_bf16 v[102:105], v[252:255], v[46:49], v[102:105]\n" \
    "v_cvt_pk_bf16_f32 v32, v74, v75\n" \
    "v_cvt_pk_bf16_f32 v33, v76, v77\n" \
    "v_mov_b64 v[18:19], 0\n" \
    "ds_read_b128 v[50:53], v110 offset:0\n" \
    "ds_read_b128 v[54:57], v110 offset:1024\n" \
    "ds_read_b128 v[58:61], v110 offset:2048\n" \
    "ds_read_b128 v[62:65], v110 offset:3072\n" \
    "ds_read_b128 v[66:69], v110 offset:14336\n" \
    "ds_read_b128 v[70:73], v110 offset:15360\n" \
    "ds_read_b128 v[74:77], v110 offset:16384\n" \
    "v_cvt_pk_bf16_f32 v36, v78, v79\n" \
    "v_cvt_pk_bf16_f32 v37, v80, v81\n" \
    "ds_read_b128 v[78:81], v110 offset:17408\n" \
    "v_cvt_pk_bf16_f32 v38, v82, v83\n" \
    "v_cvt_pk_bf16_f32 v39, v84, v85\n" \
    "v_cvt_pk_bf16_f32 v40, v86, v87\n" \
    "v_cvt_pk_bf16_f32 v41, v88, v89\n" \
    "v_cvt_pk_bf16_f32 v42, v90, v91\n" \
    "v_cvt_pk_bf16_f32 v43, v92, v93\n" \
    "v_cvt_pk_bf16_f32 v44, v94, v95\n" \
    "v_cvt_pk_bf16_f32 v45, v96, v97\n" \
    "v_cvt_pk_bf16_f32 v46, v98, v99\n" \
    "v_cvt_pk_bf16_f32 v47, v100, v101\n" \
    "v_cvt_pk_bf16_f32 v48, v102, v103\n" \
    "v_cvt_pk_bf16_f32 v49, v104, v105\n" \
    "v_mov_b64 v[34:35], 0\n" \
    "LSpacked_%=:\n" \
    "buffer_load_dword v115, v114, s[4:7], s31 offen sc1\n" \
    "s_waitcnt lgkmcnt(4)\n" \
    "v_mfma_f32_16x16x32_bf16 v[82:85], v[18:21], v[50:53], 0\n" \
    "v_add_u32 v102, 2048, v4\n" \
    "v_subrev_u32 v113, 7168, v102\n" \
    "v_mfma_f32_16x16x32_bf16 v[82:85], v[22:25], v[54:57], v[82:85]\n" \
    "v_min_u32 v102, v102, v113\n" \
    "v_add_u32 v103, 3072, v4\n" \
    "v_mfma_f32_16x16x32_bf16 v[82:85], v[26:29], v[58:61], v[82:85]\n" \
    "v_subrev_u32 v113, 7168, v103\n" \
    "v_min_u32 v103, v103, v113\n" \
    "v_mfma_f32_16x16x32_bf16 v[82:85], v[30:33], v[62:65], v[82:85]\n" \
    "v_add_u32 v104, 4096, v4\n" \
    "v_subrev_u32 v113, 7168, v104\n" \
    "s_waitcnt lgkmcnt(0)\n" \
    "v_mfma_f32_16x16x32_bf16 v[86:89], v[18:21], v[66:69], 0\n" \
    "v_min_u32 v104, v104, v113\n" \
    "v_add_u32 v105, 5120, v4\n" \
    "v_mfma_f32_16x16x32_bf16 v[86:89], v[22:25], v[70:73], v[86:89]\n" \
    "v_subrev_u32 v113, 7168, v105\n" \
    "v_min_u32 v105, v105, v113\n" \
    "v_mfma_f32_16x16x32_bf16 v[86:89], v[26:29], v[74:77], v[86:89]\n" \
    "v_add_u32 v106, 6144, v4\n" \
    "v_subrev_u32 v113, 7168, v106\n" \
    "v_mfma_f32_16x16x32_bf16 v[86:89], v[30:33], v[78:81], v[86:89]\n" \
    "v_min_u32 v106, v106, v113\n" \
    "ds_write_b128 v111, v[82:85] offset:24576\n" \
    "v_mfma_f32_16x16x32_bf16 v[90:93], v[34:37], v[66:69], 0\n" \
    "v_mov_b64 v[58:59], 0\n" \
    "v_mov_b64 v[60:61], 0\n" \
    "v_mfma_f32_16x16x32_bf16 v[90:93], v[38:41], v[70:73], v[90:93]\n" \
    "v_mov_b64 v[62:63], 0\n" \
    "v_mov_b64 v[64:65], 0\n" \
    "v_mfma_f32_16x16x32_bf16 v[90:93], v[42:45], v[74:77], v[90:93]\n" \
    "v_mfma_f32_16x16x32_bf16 v[90:93], v[46:49], v[78:81], v[90:93]\n" \
    "ds_write_b128 v111, v[86:89] offset:26624\n" \
    "v_mov_b64 v[66:67], 0\n" \
    "v_mov_b64 v[68:69], 0\n" \
    "v_mov_b64 v[70:71], 0\n" \
    "v_mov_b64 v[72:73], 0\n" \
    "v_mov_b64 v[74:75], 0\n" \
    "v_mov_b64 v[76:77], 0\n" \
    "v_mov_b64 v[78:79], 0\n" \
    "v_mov_b64 v[80:81], 0\n" \
    "ds_write_b128 v112, v[90:93] offset:26624\n" \
    "s_waitcnt lgkmcnt(0)\n" \
    "v_mov_b64 v[82:83], 0\n" \
    "v_mov_b64 v[84:85], 0\n" \
    "v_mov_b64 v[86:87], 0\n" \
    "v_mov_b64 v[88:89], 0\n" \
    "v_mov_b64 v[90:91], 0\n" \
    "v_mov_b64 v[92:93], 0\n" \
    "s_cmp_lg_u32 s14, 1\n" \
    "s_cbranch_scc1 LSgo_%=\n" \
    "s_cmp_eq_u32 s13, 0\n" \
    "s_cbranch_scc1 LSpolled_%=\n" \
    "LSpoll1_%=:\n" \
    "s_waitcnt vmcnt(0)\n" \
    "v_max_u32_dpp v115, v115, v115 quad_perm:[1,0,3,2] row_mask:0xf bank_mask:0xf\n" \
    "s_nop 1\n" \
    "v_max_u32_dpp v115, v115, v115 quad_perm:[2,3,0,1] row_mask:0xf bank_mask:0xf\n" \
    "s_nop 1\n" \
    "v_max_u32_dpp v115, v115, v115 row_half_mirror row_mask:0xf bank_mask:0xf\n" \
    "s_nop 1\n" \
    "v_max_u32_dpp v115, v115, v115 row_mirror row_mask:0xf bank_mask:0xf\n" \
    "s_nop 0\n" \
    "v_readfirstlane_b32 s30, v115\n" \
    "s_nop 0\n" \
    "s_cmp_eq_u32 s30, -1\n" \
    "s_cbranch_scc1 LSpoll_%=\n" \
    "LSpolled_%=:\n" \
    "s_and_b32 s76, s30, 7\n" \
    "s_and_b32 s24, s13, 7\n" \
    "s_cmp_eq_u32 s24, 0\n" \
    "s_subb_u32 s76, s76, 0\n" \
    "s_max_i32 s76, s76, 0\n" \
    "s_min_i32 s76, s76, 4\n" \
    "s_mul_i32 s24, s76, 17\n" \
    "s_add_u32 s24, s24, s90\n" \
    "v_writelane_b32 v108, s24, 0\n" \
    "s_add_u32 s24, s24, s91\n" \
    "v_writelane_b32 v108, s24, 2\n" \
    "s_mov_b32 s31, s30\n" \
    "s_cmp_eq_u32 s13, 0\n" \
    "s_cbranch_scc1 LSgo_%=\n" \
    "s_getpc_b64 s[36:37]\n" \
    "LSfw0_%=:\n" \
    "s_add_u32 s36, s36, LSfw9_%=-LSfw0_%=\n" \
    "s_addc_u32 s37, s37, 0\n" \
    "s_sub_u32 s31, s31, 270\n" \
    "LSfw1_%=:\n" \
    "s_memrealtime s[34:35]\n" \
    "s_sub_u32 s17, s17, 1\n" \
    "s_waitcnt lgkmcnt(0)\n" \
    "s_mul_i32 s34, s34, 24\n" \
    "s_sub_u32 s34, s31, s34\n" \
    "s_min_i32 s34, s34, s17\n" \
    "s_cmpk_gt_i32 s34, 250\n" \
    "s_cbranch_scc1 LSfw1_%=\n" \
    "s_memrealtime s[42:43]\n" \
    "s_nop 0\n" \
    "s_memrealtime s[44:45]\n" \
    "s_nop 0\n" \
    "s_memrealtime s[46:47]\n" \
    "s_nop 0\n" \
    "s_memrealtime s[48:49]\n" \
    "s_nop 0\n" \
    "s_memrealtime s[50:51]\n" \
    "s_nop 0\n" \
    "s_memrealtime s[52:53]\n" \
    "s_nop 0\n" \
    "s_memrealtime s[54:55]\n" \
    "s_nop 0\n" \
    "s_memrealtime s[56:57]\n" \
    "s_nop 0\n" \
    "s_memrealtime s[58:59]\n" \
    "s_nop 0\n" \
    "s_memrealtime s[60:61]\n" \
    "s_nop 0\n" \
    "s_memrealtime s[62:63]\n" \
    "s_nop 0\n" \
    "s_memrealtime s[64:65]\n" \
    "s_nop 0\n" \
    "s_waitcnt lgkmcnt(0)\n" \
    "s_add_u32 s42, s42, s44\n" \
    "s_add_u32 s42, s42, s46\n" \
    "s_add_u32 s42, s42, s48\n" \
    "s_add_u32 s42, s42, s50\n" \
    "s_add_u32 s42, s42, s52\n" \
    "s_add_u32 s42, s42, s54\n" \
    "s_add_u32 s42, s42, s56\n" \
    "s_add_u32 s42, s42, s58\n" \
    "s_add_u32 s42, s42, s60\n" \
    "s_add_u32 s42, s42, s62\n" \
    "s_add_u32 s42, s42, s64\n" \
    "s_lshl_b32 s42, s42, 1\n" \
    "s_sub_u32 s34, s31, s42\n" \
    "s_max_i32 s34, s34, 0\n" \
    "s_min_i32 s34, s34, 252\n" \
    "s_and_b32 s34, s34, 0xfc\n" \
    "s_sub_u32 s36, s36, s34\n" \
    "s_subb_u32 s37, s37, 0\n" \
    "s_setpc_b64 s[36:37]\n" \
    ".p2align 6\n" \
    "s_nop 0\n" \
    "s_nop 0\n" \
    "s_nop 0\n" \
    "s_nop 0\n" \
    "s_nop 0\n" \
    "s_nop 0\n" \
    "s_nop 0\n" \
    "s_nop 0\n" \
    "s_nop 0\n" \
    "s_nop 0\n" \
    "s_nop 0\n" \
    "s_nop 0\n" \
    "s_nop 0\n" \
    "s_nop 0\n" \
    "s_nop 0\n" \
    "s_nop 0\n" \
    "s_nop 0\n" \
    "s_nop 0\n" \
    "s_nop 0\n" \
    "s_nop 0\n" \
    "s_nop 0\n" \
    "s_nop 0\n" \
    "s_nop 0\n" \
    "s_nop 0\n" \
    "s_nop 0\n" \
    "s_nop 0\n" \
    "s_nop 0\n" \
    "s_nop 0\n" \
    "s_nop 0\n" \
    "s_nop 0\n" \
    "s_nop 0\n" \
    "s_nop 0\n" \
    "s_nop 0\n" \
    "s_nop 0\n" \
    "s_nop 0\n" \
    "s_nop 0\n" \
    "s_nop 0\n" \
    "s_nop 0\n" \
    "s_nop 0\n" \
    "s_nop 0\n" \
    "s_nop 0\n" \
    "s_nop 0\n" \
    "s_nop 0\n" \
    "s_nop 0\n" \
    "s_nop 0\n" \
    "s_nop 0\n" \
    "s_nop 0\n" \
    "s_nop 0\n" \
    "s_nop 0\n" \
    "s_nop 0\n" \
    "s_nop 0\n" \
    "s_nop 0\n" \
    "s_nop 0\n" \
    "s_nop 0\n" \
    "s_nop 0\n" \
    "s_nop 0\n" \
    "s_nop 0\n" \
    "s_nop 0\n" \
    "s_nop 0\n" \
    "s_nop 0\n" \
    "s_nop 0\n" \
    "s_nop 0\n" \
    "s_nop 0\n" \
    "s_nop 0\n" \
    "LSfw9_%=:\n" \
    "LSgo_%=:\n" \
    "s_barrier\n" \
    "LSgather_%=:\n" \
    "buffer_load_dwordx4 v[50:53], v0, s[4:7], s21 offen sc1\n" \
    "s_nop 0\n" \
    "buffer_load_dwordx4 v[54:57], v0, s[4:7], s21 offen offset:1024 sc1\n" \
    "s_mov_b32 s90, 0xf0f0f0f0\n" \
    "s_mov_b32 s91, s90\n" \
    "s_cmp_lg_u32 s14, 1\n" \
    "s_cbranch_scc1 LSissued_%=\n" \
    "LSanr_%=:\n" \
    "s_mov_b32 s36, 1\n" \
    "LSanr1_%=:\n" \
    "s_memtime s[40:41]\n" \
    "s_nop 0\n" \
    "s_memrealtime s[42:43]\n" \
    "s_nop 0\n" \
    "s_memrealtime s[44:45]\n" \
    "s_nop 0\n" \
    "s_memrealtime s[46:47]\n" \
    "s_nop 0\n" \
    "s_memrealtime s[48:49]\n" \
    "s_nop 0\n" \
    "s_memrealtime s[50:51]\n" \
    "s_nop 0\n" \
    "s_memrealtime s[52:53]\n" \
    "s_nop 0\n" \
    "s_memrealtime s[54:55]\n" \
    "s_nop 0\n" \
    "s_memrealtime s[56:57]\n" \
    "s_nop 0\n" \
    "s_memrealtime s[58:59]\n" \
    "s_nop 0\n" \
    "s_memrealtime s[60:61]\n" \
    "s_nop 0\n" \
    "s_memrealtime s[62:63]\n" \
    "s_nop 0\n" \
    "s_memrealtime s[64:65]\n" \
    "s_waitcnt lgkmcnt(0)\n" \
    "s_add_u32 s42, s42, s44\n" \
    "s_add_u32 s42, s42, s46\n" \
    "s_add_u32 s42, s42, s48\n" \
    "s_add_u32 s42, s42, s50\n" \
    "s_add_u32 s42, s42, s52\n" \
    "s_add_u32 s42, s42, s54\n" \
    "s_add_u32 s42, s42, s56\n" \
    "s_add_u32 s42, s42, s58\n" \
    "s_add_u32 s42, s42, s60\n" \
    "s_add_u32 s42, s42, s62\n" \
    "s_add_u32 s42, s42, s64\n" \
    "s_lshl_b32 s42, s42, 1\n" \
    "s_sub_u32 s18, s42, s40\n" \
    "s_ashr_i32 s38, s19, 3\n" \
    "s_add_u32 s38, s38, s19\n" \
    "s_add_u32 s38, s38, s18\n" \
    "s_add_u32 s38, s38, s80\n" \
    "s_sub_u32 s29, s30, s18\n" \
    "s_add_u32 s29, s29, s83\n" \
    "s_cmp_eq_u32 s13, 0\n" \
    "s_cselect_b32 s29, s40, s29\n" \
    "s_cselect_b32 s3, 0, 34\n" \
    "s_and_b32 s38, s38, -8\n" \
    "s_add_u32 s3, s3, s84\n" \
    "s_add_u32 s3, s3, s38\n" \
    ".p2align 5\n" \
    "s_nop 0\n" \
    "s_nop 0\n" \
    "s_nop 0\n" \
    "s_nop 0\n" \
    "LSissued_%=:\n" \
    "s_waitcnt vmcnt(1)\n" \
    "v_smfmac_f32_16x16x64_bf16 v[86:89], v[50:53], v[18:25], v1\n" \
    "ds_write_b128 v4, v[50:53]\n" \
    "v_smfmac_f32_16x16x64_bf16 v[90:93], v[50:53], v[34:41], v1\n" \
    "v_smfmac_f32_16x16x64_bf16 v[58:61], v[50:53], a[0:7], v1\n" \
    "v_max3_u32 v110, v50, v51, v52\n" \
    "v_smfmac_f32_16x16x64_bf16 v[62:65], v[50:53], a[8:15], v1\n" \
    "s_waitcnt vmcnt(0)\n" \
    "ds_write_b128 v4, v[54:57] offset:1024\n" \
    "v_smfmac_f32_16x16x64_bf16 v[86:89], v[54:57], v[26:33], v1\n" \
    "v_max3_u32 v110, v110, v53, v54\n" \
    "v_smfmac_f32_16x16x64_bf16 v[90:93], v[54:57], v[42:49], v1\n" \
    "v_max3_u32 v110, v110, v55, v56\n" \
    "v_smfmac_f32_16x16x64_bf16 v[66:69], v[50:53], a[16:23], v1\n" \
    "v_max_u32 v110, v110, v57\n" \
    "v_smfmac_f32_16x16x64_bf16 v[70:73], v[50:53], a[24:31], v1\n" \
    "v_cmp_eq_u32 vcc, -1, v110\n" \
    "v_smfmac_f32_16x16x64_bf16 v[74:77], v[50:53], a[32:39], v1\n" \
    "s_nop 0\n" \
    "v_add_f32 v110, v86, v87\n" \
    "v_smfmac_f32_16x16x64_bf16 v[78:81], v[50:53], a[40:47], v1\n" \
    "s_cbranch_vccnz LSretry_%=\n" \
    "LSchecked_%=:\n" \
    "v_smfmac_f32_16x16x64_bf16 v[82:85], v[50:53], a[48:55], v1\n" \
    "s_waitcnt lgkmcnt(0)\n" \
    "s_barrier\n" \
    "v_add_f32 v112, v88, v89\n" \
    "v_smfmac_f32_16x16x64_bf16 v[58:61], v[54:57], a[56:63], v1\n" \
    "ds_read_b128 v[18:21], v102\n" \
    "v_add_f32 v111, v90, v91\n" \
    "v_smfmac_f32_16x16x64_bf16 v[62:65], v[54:57], a[64:71], v1\n" \
    "s_memtime s[34:35]\n" \
    "v_add_f32 v113, v92, v93\n" \
    "v_smfmac_f32_16x16x64_bf16 v[66:69], v[54:57], a[72:79], v1\n" \
    "ds_write_b32 v5, v110 offset:16384\n" \
    "v_smfmac_f32_16x16x64_bf16 v[70:73], v[54:57], a[80:87], v1\n" \
    "ds_write_b32 v5, v112 offset:16896\n" \
    "v_smfmac_f32_16x16x64_bf16 v[74:77], v[54:57], a[88:95], v1\n" \
    "ds_write_b32 v5, v111 offset:16640\n" \
    "v_smfmac_f32_16x16x64_bf16 v[78:81], v[54:57], a[96:103], v1\n" \
    "ds_write_b32 v5, v113 offset:17152\n" \
    "s_cmp_gt_u32 s34, s29\n" \
    "s_cselect_b32 s24, s3, s38\n" \
    "v_smfmac_f32_16x16x64_bf16 v[82:85], v[54:57], a[104:111], v1\n" \
    "s_waitcnt lgkmcnt(4)\n" \
    "s_barrier\n" \
    "s_and_b32 s88, s34, -8\n" \
    "v_smfmac_f32_16x16x64_bf16 v[58:61], v[18:21], a[112:119], v1\n" \
    "ds_read_b128 v[38:41], v6 offset:16384\n" \
    "s_add_u32 s88, s88, s24\n" \
    "v_smfmac_f32_16x16x64_bf16 v[62:65], v[18:21], a[120:127], v1\n" \
    "ds_read_b128 v[22:25], v103\n" \
    "v_add_u32 v46, s88, v108\n" \
    "v_smfmac_f32_16x16x64_bf16 v[66:69], v[18:21], a[128:135], v1\n" \
    "buffer_store_dword v46, v14, s[4:7], 0 offen sc1\n" \
    "v_smfmac_f32_16x16x64_bf16 v[70:73], v[18:21], a[136:143], v1\n" \
    "ds_read_b128 v[26:29], v104\n" \
    "v_smfmac_f32_16x16x64_bf16 v[74:77], v[18:21], a[144:151], v1\n" \
    "s_waitcnt lgkmcnt(2)\n" \
    "ds_read_b128 v[30:33], v105\n" \
    "v_add_f32 v44, v38, v39\n" \
    "v_smfmac_f32_16x16x64_bf16 v[78:81], v[18:21], a[152:159], v1\n" \
    "ds_read_b128 v[34:37], v106\n" \
    "v_add_f32 v45, v40, v41\n" \
    "v_smfmac_f32_16x16x64_bf16 v[82:85], v[18:21], a[160:167], v1\n" \
    "s_waitcnt lgkmcnt(3)\n" \
    "ds_read2_b32 v[94:95], v13 offset0:0 offset1:4\n" \
    "v_add_f32 v38, v44, v45\n" \
    "v_smfmac_f32_16x16x64_bf16 v[58:61], v[22:25], a[168:175], v1\n" \
    "s_waitcnt lgkmcnt(3)\n" \
    "v_cmp_nle_f32 vcc, |v38|, s28\n" \
    "v_smfmac_f32_16x16x64_bf16 v[58:61], v[26:29], a[224:231], v1\n" \
    "s_waitcnt lgkmcnt(2)\n" \
    "v_exp_f32 v38, v38\n" \
    "v_smfmac_f32_16x16x64_bf16 v[58:61], v[30:33], v[144:151], v1\n" \
    "s_waitcnt lgkmcnt(1)\n" \
    "v_mov_b32_dpp v39, v38 quad_perm:[1,0,3,2] row_mask:0xf bank_mask:0xf\n" \
    "v_smfmac_f32_16x16x64_bf16 v[58:61], v[34:37], v[200:207], v1\n" \
    "ds_read2_b32 v[96:97], v13 offset0:128 offset1:132\n" \
    "v_cvt_pk_bf16_f32 v40, |v38|, |v39|\n" \
    "v_smfmac_f32_16x16x64_bf16 v[62:65], v[22:25], a[176:183], v1\n" \
    "s_cbranch_vccnz LSxmax_%=\n" \
    "LSxback_%=:\n" \
    "buffer_store_dword v40, v109, s[4:7], s23 offen sc1\n" \
    "v_smfmac_f32_16x16x64_bf16 v[62:65], v[26:29], a[232:239], v1\n" \
    "s_nop 1\n" \
    "v_smfmac_f32_16x16x64_bf16 v[62:65], v[30:33], v[152:159], v1\n" \
    "v_add_f32 v58, v58, v59\n" \
    "v_smfmac_f32_16x16x64_bf16 v[62:65], v[34:37], v[208:215], v1\n" \
    "s_nop 1\n" \
    "v_add_f32 v60, v60, v61\n" \
    "v_smfmac_f32_16x16x64_bf16 v[66:69], v[22:25], a[184:191], v1\n" \
    "v_cvt_pk_bf16_f32 v114, v58, v60\n" \
    "v_smfmac_f32_16x16x64_bf16 v[66:69], v[26:29], a[240:247], v1\n" \
    "ds_write_b32 v7, v114 offset:0\n" \
    "v_smfmac_f32_16x16x64_bf16 v[66:69], v[30:33], v[160:167], v1\n" \
    "ds_read2_b32 v[98:99], v107 offset0:0 offset1:4\n" \
    "v_add_f32 v62, v62, v63\n" \
    "v_smfmac_f32_16x16x64_bf16 v[66:69], v[34:37], v[216:223], v1\n" \
    "s_nop 1\n" \
    "v_add_f32 v64, v64, v65\n" \
    "v_smfmac_f32_16x16x64_bf16 v[70:73], v[22:25], a[192:199], v1\n" \
    "v_cvt_pk_bf16_f32 v115, v62, v64\n" \
    "v_smfmac_f32_16x16x64_bf16 v[70:73], v[26:29], a[248:255], v1\n" \
    "ds_write_b32 v7, v115 offset:4\n" \
    "v_smfmac_f32_16x16x64_bf16 v[70:73], v[30:33], v[168:175], v1\n" \
    "ds_read2_b32 v[100:101], v2 offset0:0 offset1:4\n" \
    "v_add_f32 v66, v66, v67\n" \
    "v_smfmac_f32_16x16x64_bf16 v[70:73], v[34:37], v[224:231], v1\n" \
    "ds_read_b64_tr_b16 v[102:103], v8\n" \
    "v_add_f32 v68, v68, v69\n" \
    "v_smfmac_f32_16x16x64_bf16 v[74:77], v[22:25], a[200:207], v1\n" \
    "s_nop 1\n" \
    "v_cvt_pk_bf16_f32 v114, v66, v68\n" \
    "v_smfmac_f32_16x16x64_bf16 v[74:77], v[26:29], v[120:127], v1\n" \
    "ds_write_b32 v7, v114 offset:0\n" \
    "v_smfmac_f32_16x16x64_bf16 v[74:77], v[30:33], v[176:183], v1\n" \
    "v_add_f32 v70, v70, v71\n" \
    "v_smfmac_f32_16x16x64_bf16 v[74:77], v[34:37], v[232:239], v1\n" \
    "s_nop 1\n" \
    "v_add_f32 v72, v72, v73\n" \
    "v_smfmac_f32_16x16x64_bf16 v[78:81], v[22:25], a[208:215], v1\n" \
    "v_cvt_pk_bf16_f32 v115, v70, v72\n" \
    "v_smfmac_f32_16x16x64_bf16 v[78:81], v[26:29], v[128:135], v1\n" \
    "ds_write_b32 v7, v115 offset:4\n" \
    "v_smfmac_f32_16x16x64_bf16 v[78:81], v[30:33], v[184:191], v1\n" \
    "v_add_f32 v74, v74, v75\n" \
    "v_smfmac_f32_16x16x64_bf16 v[78:81], v[34:37], v[240:247], v1\n" \
    "ds_read_b64_tr_b16 v[104:105], v8\n" \
    "v_add_f32 v76, v76, v77\n" \
    "v_smfmac_f32_16x16x64_bf16 v[82:85], v[22:25], a[216:223], v1\n" \
    "v_cvt_pk_bf16_f32 v114, v74, v76\n" \
    "v_smfmac_f32_16x16x64_bf16 v[82:85], v[26:29], v[136:143], v1\n" \
    "ds_write_b32 v7, v114 offset:0\n" \
    "v_smfmac_f32_16x16x64_bf16 v[82:85], v[30:33], v[192:199], v1\n" \
    "s_cmp_lg_u32 s1, 0\n" \
    "v_cvt_pk_bf16_f32 v110, v78, v79\n" \
    "v_smfmac_f32_16x16x64_bf16 v[82:85], v[34:37], v[248:255], v1\n" \
    "ds_read_b64_tr_b16 v[106:107], v8\n" \
    "v_cvt_pk_bf16_f32 v111, v80, v81\n" \
    "s_mov_b64 vcc, s[90:91]\n" \
    "s_nop 0\n" \
    "v_add_f32 v43, v38, v39\n" \
    "v_add_u32 v114, s0, v0\n" \
    "s_nop 0\n" \
    "v_add_f32_dpp v43, v43, v43 quad_perm:[2,3,0,1] row_mask:0xf bank_mask:0xf\n" \
    "v_cvt_pk_bf16_f32 v112, v82, v83\n" \
    "v_cvt_pk_bf16_f32 v113, v84, v85\n" \
    "ds_write_b32 v8, v43 offset:1540\n" \
    "s_nop 0\n" \
    "v_mfma_f32_16x16x32_bf16 v[90:93], v[110:113], v[116:119], 0\n" \
    "s_waitcnt lgkmcnt(1)\n" \
    "s_nop 1\n" \
    "v_mfma_f32_16x16x32_bf16 v[86:89], v[102:105], v[94:97], 0\n" \
    "s_nop 3\n" \
    "v_cvt_pk_bf16_f32 v108, v90, v91\n" \
    "v_cvt_pk_bf16_f32 v109, v92, v93\n" \
    "s_nop 1\n" \
    "v_mfma_f32_16x16x32_bf16 v[86:89], v[106:109], v[98:101], v[86:89]\n" \
    "s_barrier\n" \
    "s_nop 0\n" \
    "s_nop 0\n" \
    "v_and_b32 v111, 56, v17\n" \
    "v_lshlrev_b32 v111, 1, v111\n" \
    "s_nop 0\n" \
    "v_xor_b32 v111, v111, v6\n" \
    "s_nop 0\n" \
    "v_add_f32_dpp v110, v88, v86 row_ror:8 row_mask:0xf bank_mask:0x3\n" \
    "v_add_f32_dpp v110, v87, v89 row_ror:8 row_mask:0xf bank_mask:0xc\n" \
    "ds_write_b32 v9, v110\n" \
    "s_cbranch_scc1 LSother_%=\n" \
    "ds_read_b128 v[58:61], v0 offset:15360\n" \
    "ds_read_b64 v[66:67], v12\n" \
    "s_waitcnt lgkmcnt(1)\n" \
    "v_add_f32 v58, v58, v59\n" \
    "v_add_f32 v60, v60, v61\n" \
    "s_waitcnt lgkmcnt(0)\n" \
    "v_sub_f32 v58, v58, v66\n" \
    "v_add_f32 v58, v58, v60\n" \
    "v_exp_f32 v59, v58\n" \
    "v_cmp_nle_f32 s[26:27], v58, s28\n" \
    "v_add_f32 v70, v59, v67\n" \
    "v_cndmask_b32 v71, v59, v15, vcc\n" \
    "v_cndmask_b32_dpp v72, v59, v16, vcc row_half_mirror row_mask:0xf bank_mask:0xf\n" \
    "v_add_f32_dpp v70, v70, v70 quad_perm:[1,0,3,2] row_mask:0xf bank_mask:0xf\n" \
    "s_cmp_lg_u64 s[26:27], 0\n" \
    "s_cbranch_scc1 LSmmax_%=\n" \
    "v_add_f32_dpp v70, v70, v70 quad_perm:[2,3,0,1] row_mask:0xf bank_mask:0xf\n" \
    "s_nop 1\n" \
    "v_add_f32_dpp v70, v70, v70 row_half_mirror row_mask:0xf bank_mask:0xf\n" \
    "v_rcp_f32 v70, v70\n" \
    "s_nop 0\n" \
    "v_mul_f32 v71, v71, v70\n" \
    "v_mul_f32 v72, v72, v70\n" \
    "v_cvt_pk_bf16_f32 v71, |v71|, |v72|\n" \
    "buffer_store_dword v71, v10, s[4:7], s22 offen sc1\n" \
    "s_branch LSpost_%=\n" \
    "LSretry_%=:\n" \
    "s_add_u32 s39, s39, 1\n" \
    "s_sub_u32 s17, s17, 1\n" \
    "s_cmp_lt_i32 s17, 0\n" \
    "s_cbranch_scc1 LSchecked_%=\n" \
    "s_nop 3\n" \
    "buffer_load_dwordx4 v[50:53], v0, s[4:7], s21 offen sc1\n" \
    "buffer_load_dwordx4 v[54:57], v0, s[4:7], s21 offen offset:1024 sc1\n" \
    "v_mov_b32 v58, 0\n" \
    "v_mov_b32 v59, 0\n" \
    "v_mov_b32 v60, 0\n" \
    "v_mov_b32 v61, 0\n" \
    "v_mov_b32 v62, 0\n" \
    "v_mov_b32 v63, 0\n" \
    "v_mov_b32 v64, 0\n" \
    "v_mov_b32 v65, 0\n" \
    "v_mov_b32 v66, 0\n" \
    "v_mov_b32 v67, 0\n" \
    "v_mov_b32 v68, 0\n" \
    "v_mov_b32 v69, 0\n" \
    "v_mov_b32 v70, 0\n" \
    "v_mov_b32 v71, 0\n" \
    "v_mov_b32 v72, 0\n" \
    "v_mov_b32 v73, 0\n" \
    "v_mov_b32 v74, 0\n" \
    "v_mov_b32 v75, 0\n" \
    "v_mov_b32 v76, 0\n" \
    "v_mov_b32 v77, 0\n" \
    "v_mov_b32 v78, 0\n" \
    "v_mov_b32 v79, 0\n" \
    "v_mov_b32 v80, 0\n" \
    "v_mov_b32 v81, 0\n" \
    "v_mov_b32 v82, 0\n" \
    "v_mov_b32 v83, 0\n" \
    "v_mov_b32 v84, 0\n" \
    "v_mov_b32 v85, 0\n" \
    "v_mov_b32 v86, 0\n" \
    "v_mov_b32 v87, 0\n" \
    "v_mov_b32 v88, 0\n" \
    "v_mov_b32 v89, 0\n" \
    "v_mov_b32 v90, 0\n" \
    "v_mov_b32 v91, 0\n" \
    "v_mov_b32 v92, 0\n" \
    "v_mov_b32 v93, 0\n" \
    "s_branch LSissued_%=\n" \
    "LSxmax_%=:\n" \
    "ds_read_b128 v[38:41], v6 offset:16384\n" \
    "s_mov_b32 s24, 0x7fc0dead\n" \
    "s_waitcnt lgkmcnt(0)\n" \
    "v_cmp_eq_u32 vcc, s24, v38\n" \
    "s_cbranch_vccnz LSxmax_%=\n" \
    "v_cmp_eq_u32 vcc, s24, v39\n" \
    "s_cbranch_vccnz LSxmax_%=\n" \
    "v_cmp_eq_u32 vcc, s24, v40\n" \
    "s_cbranch_vccnz LSxmax_%=\n" \
    "v_cmp_eq_u32 vcc, s24, v41\n" \
    "s_cbranch_vccnz LSxmax_%=\n" \
    "v_add_f32 v44, v38, v39\n" \
    "v_add_f32 v45, v40, v41\n" \
    "v_add_f32 v38, v44, v45\n" \
    "v_cmp_nle_f32 vcc, |v38|, s28\n" \
    "s_cbranch_vccz LSxexp_%=\n" \
    "v_max_f32_dpp v42, v38, v38 quad_perm:[1,0,3,2] row_mask:0xf bank_mask:0xf\n" \
    "s_nop 1\n" \
    "v_max_f32_dpp v42, v42, v42 quad_perm:[2,3,0,1] row_mask:0xf bank_mask:0xf\n" \
    "s_nop 1\n" \
    "v_max_f32_dpp v42, v42, v42 row_half_mirror row_mask:0xf bank_mask:0xf\n" \
    "s_nop 1\n" \
    "v_max_f32_dpp v42, v42, v42 row_mirror row_mask:0xf bank_mask:0xf\n" \
    "v_mov_b32 v39, v42\n" \
    "s_nop 1\n" \
    "v_permlane16_swap_b32 v42, v39\n" \
    "v_max_f32 v42, v42, v39\n" \
    "v_sub_f32 v38, v38, v42\n" \
    "ds_write_b32 v8, v42 offset:1536\n" \
    "LSxexp_%=:\n" \
    "v_exp_f32 v38, v38\n" \
    "s_nop 1\n" \
    "v_mov_b32_dpp v39, v38 quad_perm:[1,0,3,2] row_mask:0xf bank_mask:0xf\n" \
    "v_cvt_pk_bf16_f32 v40, |v38|, |v39|\n" \
    "s_branch LSxback_%=\n" \
    "LSmmax_%=:\n" \
    "ds_read_b128 v[58:61], v0 offset:15360\n" \
    "s_mov_b32 s24, 0x7fc0dead\n" \
    "s_waitcnt lgkmcnt(0)\n" \
    "v_cmp_eq_u32 s[26:27], s24, v58\n" \
    "s_nop 0\n" \
    "s_cmp_lg_u64 s[26:27], 0\n" \
    "s_cbranch_scc1 LSmmax_%=\n" \
    "v_cmp_eq_u32 s[26:27], s24, v59\n" \
    "s_nop 0\n" \
    "s_cmp_lg_u64 s[26:27], 0\n" \
    "s_cbranch_scc1 LSmmax_%=\n" \
    "v_cmp_eq_u32 s[26:27], s24, v60\n" \
    "s_nop 0\n" \
    "s_cmp_lg_u64 s[26:27], 0\n" \
    "s_cbranch_scc1 LSmmax_%=\n" \
    "v_cmp_eq_u32 s[26:27], s24, v61\n" \
    "s_nop 0\n" \
    "s_cmp_lg_u64 s[26:27], 0\n" \
    "s_cbranch_scc1 LSmmax_%=\n" \
    "ds_read_b128 v[58:61], v0 offset:15360\n" \
    "ds_read_b64 v[66:67], v12\n" \
    "s_waitcnt lgkmcnt(1)\n" \
    "v_add_f32 v58, v58, v59\n" \
    "v_add_f32 v60, v60, v61\n" \
    "s_waitcnt lgkmcnt(0)\n" \
    "v_sub_f32 v58, v58, v66\n" \
    "v_add_f32 v58, v58, v60\n" \
    "s_nop 1\n" \
    "v_max_f32_dpp v68, v58, v58 quad_perm:[1,0,3,2] row_mask:0xf bank_mask:0xf\n" \
    "s_nop 1\n" \
    "v_max_f32_dpp v68, v68, v68 quad_perm:[2,3,0,1] row_mask:0xf bank_mask:0xf\n" \
    "s_nop 1\n" \
    "v_max_f32_dpp v68, v68, v68 row_half_mirror row_mask:0xf bank_mask:0xf\n" \
    "v_cmp_nle_f32 s[26:27], v58, s28\n" \
    "v_max_f32 v68, 0, v68\n" \
    "s_cmp_lg_u64 s[26:27], 0\n" \
    "s_cselect_b32 s24, -1, 0\n" \
    "v_and_b32 v68, s24, v68\n" \
    "v_sub_f32 v58, v58, v68\n" \
    "v_exp_f32 v59, v58\n" \
    "v_exp_f32 v69, -v68\n" \
    "s_nop 0\n" \
    "v_fma_f32 v70, v69, v67, v59\n" \
    "v_mul_f32 v71, v69, v15\n" \
    "v_mul_f32 v72, v69, v16\n" \
    "v_add_f32_dpp v70, v70, v70 quad_perm:[1,0,3,2] row_mask:0xf bank_mask:0xf\n" \
    "s_nop 1\n" \
    "v_add_f32_dpp v70, v70, v70 quad_perm:[2,3,0,1] row_mask:0xf bank_mask:0xf\n" \
    "s_nop 1\n" \
    "v_add_f32_dpp v70, v70, v70 row_half_mirror row_mask:0xf bank_mask:0xf\n" \
    "v_cndmask_b32 v71, v59, v71, vcc\n" \
    "v_cndmask_b32_dpp v72, v59, v72, vcc row_half_mirror row_mask:0xf bank_mask:0xf\n" \
    "v_rcp_f32 v70, v70\n" \
    "s_nop 0\n" \
    "v_mul_f32 v71, v71, v70\n" \
    "v_mul_f32 v72, v72, v70\n" \
    "v_cvt_pk_bf16_f32 v71, |v71|, |v72|\n" \
    "buffer_store_dword v71, v10, s[4:7], s22 offen sc1\n" \
    "s_branch LSpost_%=\n" \
    "LSnap_%=:\n" \
    "s_nop 11\n" \
    "LSnap1_%=:\n" \
    "s_waitcnt lgkmcnt(0)\n" \
    "s_branch LSpost_%=\n" \
    "LSother_%=:\n" \
    "s_cmp_eq_u32 s13, 63\n" \
    "s_cbranch_scc1 LSnap1_%=\n" \
    "s_cmp_lg_u32 s14, 1\n" \
    "s_cbranch_scc1 LSnap_%=\n" \
    "s_mov_b32 s10, s18\n" \
    "LSanp_%=:\n" \
    "s_mov_b32 s36, 1\n" \
    "LSanp1_%=:\n" \
    "s_memtime s[40:41]\n" \
    "s_nop 0\n" \
    "s_memrealtime s[42:43]\n" \
    "s_nop 0\n" \
    "s_memrealtime s[44:45]\n" \
    "s_nop 0\n" \
    "s_memrealtime s[46:47]\n" \
    "s_nop 0\n" \
    "s_memrealtime s[48:49]\n" \
    "s_nop 0\n" \
    "s_memrealtime s[50:51]\n" \
    "s_nop 0\n" \
    "s_memrealtime s[52:53]\n" \
    "s_nop 0\n" \
    "s_memrealtime s[54:55]\n" \
    "s_nop 0\n" \
    "s_memrealtime s[56:57]\n" \
    "s_nop 0\n" \
    "s_memrealtime s[58:59]\n" \
    "s_nop 0\n" \
    "s_memrealtime s[60:61]\n" \
    "s_nop 0\n" \
    "s_memrealtime s[62:63]\n" \
    "s_nop 0\n" \
    "s_memrealtime s[64:65]\n" \
    "s_waitcnt lgkmcnt(0)\n" \
    "s_add_u32 s42, s42, s44\n" \
    "s_add_u32 s42, s42, s46\n" \
    "s_add_u32 s42, s42, s48\n" \
    "s_add_u32 s42, s42, s50\n" \
    "s_add_u32 s42, s42, s52\n" \
    "s_add_u32 s42, s42, s54\n" \
    "s_add_u32 s42, s42, s56\n" \
    "s_add_u32 s42, s42, s58\n" \
    "s_add_u32 s42, s42, s60\n" \
    "s_add_u32 s42, s42, s62\n" \
    "s_add_u32 s42, s42, s64\n" \
    "s_lshl_b32 s42, s42, 1\n" \
    "s_sub_u32 s18, s42, s40\n" \
    "s_sub_u32 s24, s18, s10\n" \
    "s_max_i32 s24, s24, -200\n" \
    "s_min_i32 s24, s24, 400\n" \
    "s_cmp_lt_u32 s13, 4\n" \
    "s_cselect_b32 s24, s19, s24\n" \
    "s_add_u32 s19, s19, s24\n" \
    "s_ashr_i32 s19, s19, 1\n" \
    "s_mov_b32 s18, s10\n" \
    "v_mov_b32 v112, 0x40000000\n" \
    "v_writelane_b32 v112, s89, 0\n" \
    "buffer_store_dword v1, v112, s[4:7], 0 offen sc1\n" \
    "LSpost_%=:\n" \
    "ds_read_b128 v[18:21], v114 offset:0\n" \
    "ds_read_b128 v[22:25], v114 offset:1024\n" \
    "ds_read_b128 v[26:29], v114 offset:2048\n" \
    "ds_read_b128 v[30:33], v114 offset:3072\n" \
    "ds_read_b128 v[34:37], v114 offset:14336\n" \
    "ds_read_b128 v[38:41], v114 offset:15360\n" \
    "ds_read_b128 v[42:45], v114 offset:16384\n" \
    "ds_read_b128 v[46:49], v114 offset:17408\n" \
    "ds_read_b128 v[50:53], v111 offset:24576\n" \
    "ds_read_b128 v[54:57], v111 offset:28672\n" \
    "ds_read_b128 v[58:61], v111 offset:32768\n" \
    "ds_read_b128 v[62:65], v111 offset:36864\n" \
    "s_lshl_b32 s24, s13, 4\n" \
    "s_add_u32 s24, s24, s12\n" \
    "s_lshl_b32 s11, s24, 6\n" \
    "s_add_u32 s11, s11, 24117248\n" \
    "s_lshl_b32 s25, s13, 14\n" \
    "s_add_u32 s25, s25, s2\n" \
    "s_lshl_b32 s24, s13, 10\n" \
    "s_and_b32 s24, s24, 0x4000\n" \
    "s_xor_b32 s24, s24, s87\n" \
    "s_lshl_b32 s27, s13, 15\n" \
    "s_add_u32 s24, s24, s27\n" \
    "s_bitcmp1_b32 s14, 0\n" \
    "s_cbranch_scc1 LSpodd_%=\n" \
    "s_waitcnt lgkmcnt(10)\n" \
    "v_mfma_f32_16x16x16_bf16 v[74:77], v[102:103], v[18:19], 0\n" \
    "v_and_b32 v110, 3, v17\n" \
    "v_lshlrev_b32 v110, 8, v110\n" \
    "v_mfma_f32_16x16x16_bf16 v[78:81], v[102:103], v[20:21], 0\n" \
    "v_and_b32 v112, 60, v17\n" \
    "v_lshl_add_u32 v110, v112, 1, v110\n" \
    "v_mfma_f32_16x16x16_bf16 v[74:77], v[104:105], v[22:23], v[74:77]\n" \
    "v_mov_b32 v113, 0xceabfb8f\n" \
    "v_and_b32 v115, 0x4000000c, v11\n" \
    "s_waitcnt lgkmcnt(8)\n" \
    "v_mfma_f32_16x16x16_bf16 v[78:81], v[104:105], v[24:25], v[78:81]\n" \
    "v_lshrrev_b32 v112, 3, v17\n" \
    "v_lshlrev_b32 v112, 2, v112\n" \
    "v_mfma_f32_16x16x16_bf16 v[74:77], v[106:107], v[26:27], v[74:77]\n" \
    "v_lshl_or_b32 v112, v115, 4, v112\n" \
    "v_and_b32 v115, 0x40000000, v115\n" \
    "v_mfma_f32_16x16x16_bf16 v[74:77], v[108:109], v[28:29], v[74:77]\n" \
    "v_or_b32 v115, v115, v112\n" \
    "s_lshl_b32 s26, s14, 8\n" \
    "v_mfma_f32_16x16x16_bf16 v[78:81], v[108:109], v[30:31], v[78:81]\n" \
    "s_add_u32 s26, s26, 7680\n" \
    "s_lshl_b32 s27, s14, 4\n" \
    "s_waitcnt lgkmcnt(4)\n" \
    "v_mfma_f32_16x16x16_bf16 v[82:85], v[102:103], v[34:35], 0\n" \
    "s_and_b32 s27, s27, 16\n" \
    "s_or_b32 s26, s26, s27\n" \
    "v_mfma_f32_16x16x16_bf16 v[86:89], v[102:103], v[36:37], 0\n" \
    "v_xor_b32 v112, s26, v10\n" \
    "s_waitcnt lgkmcnt(0)\n" \
    "v_mfma_f32_16x16x16_bf16 v[82:85], v[104:105], v[38:39], v[82:85]\n" \
    "v_add_f32 v50, v50, v54\n" \
    "v_add_f32 v58, v58, v62\n" \
    "v_mfma_f32_16x16x16_bf16 v[86:89], v[104:105], v[40:41], v[86:89]\n" \
    "v_add_f32 v51, v51, v55\n" \
    "v_add_f32 v59, v59, v63\n" \
    "v_mfma_f32_16x16x16_bf16 v[82:85], v[106:107], v[42:43], v[82:85]\n" \
    "v_add_f32 v52, v52, v56\n" \
    "v_add_f32 v60, v60, v64\n" \
    "v_mfma_f32_16x16x16_bf16 v[82:85], v[108:109], v[44:45], v[82:85]\n" \
    "v_add_f32 v53, v53, v57\n" \
    "v_add_f32 v61, v61, v65\n" \
    "v_mfma_f32_16x16x16_bf16 v[86:89], v[108:109], v[46:47], v[86:89]\n" \
    "v_add_f32 v74, v74, v80\n" \
    "v_add_f32 v75, v75, v81\n" \
    "ds_write_b64 v7, v[74:75] offset:512\n" \
    "s_branch LSpeven_%=\n" \
    "LSpodd_%=:\n" \
    "s_waitcnt lgkmcnt(10)\n" \
    "v_mfma_f32_16x16x16_bf16 v[74:77], v[102:103], v[20:21], 0\n" \
    "v_and_b32 v110, 3, v17\n" \
    "v_lshlrev_b32 v110, 8, v110\n" \
    "v_mfma_f32_16x16x16_bf16 v[78:81], v[102:103], v[22:23], 0\n" \
    "v_and_b32 v112, 60, v17\n" \
    "v_lshl_add_u32 v110, v112, 1, v110\n" \
    "v_mfma_f32_16x16x16_bf16 v[74:77], v[104:105], v[24:25], v[74:77]\n" \
    "v_mov_b32 v113, 0xceabfb8f\n" \
    "v_and_b32 v115, 0x4000000c, v11\n" \
    "s_waitcnt lgkmcnt(8)\n" \
    "v_mfma_f32_16x16x16_bf16 v[78:81], v[104:105], v[26:27], v[78:81]\n" \
    "v_lshrrev_b32 v112, 3, v17\n" \
    "v_lshlrev_b32 v112, 2, v112\n" \
    "v_mfma_f32_16x16x16_bf16 v[74:77], v[106:107], v[28:29], v[74:77]\n" \
    "v_lshl_or_b32 v112, v115, 4, v112\n" \
    "v_and_b32 v115, 0x40000000, v115\n" \
    "v_mfma_f32_16x16x16_bf16 v[74:77], v[108:109], v[30:31], v[74:77]\n" \
    "v_or_b32 v115, v115, v112\n" \
    "s_lshl_b32 s26, s14, 8\n" \
    "v_mfma_f32_16x16x16_bf16 v[78:81], v[108:109], v[32:33], v[78:81]\n" \
    "s_add_u32 s26, s26, 7680\n" \
    "s_lshl_b32 s27, s14, 4\n" \
    "s_waitcnt lgkmcnt(4)\n" \
    "v_mfma_f32_16x16x16_bf16 v[82:85], v[102:103], v[36:37], 0\n" \
    "s_and_b32 s27, s27, 16\n" \
    "s_or_b32 s26, s26, s27\n" \
    "v_mfma_f32_16x16x16_bf16 v[86:89], v[102:103], v[38:39], 0\n" \
    "v_xor_b32 v112, s26, v10\n" \
    "s_waitcnt lgkmcnt(0)\n" \
    "v_mfma_f32_16x16x16_bf16 v[82:85], v[104:105], v[40:41], v[82:85]\n" \
    "v_add_f32 v50, v50, v54\n" \
    "v_add_f32 v58, v58, v62\n" \
    "v_mfma_f32_16x16x16_bf16 v[86:89], v[104:105], v[42:43], v[86:89]\n" \
    "v_add_f32 v51, v51, v55\n" \
    "v_add_f32 v59, v59, v63\n" \
    "v_mfma_f32_16x16x16_bf16 v[82:85], v[106:107], v[44:45], v[82:85]\n" \
    "v_add_f32 v52, v52, v56\n" \
    "v_add_f32 v60, v60, v64\n" \
    "v_mfma_f32_16x16x16_bf16 v[82:85], v[108:109], v[46:47], v[82:85]\n" \
    "v_add_f32 v53, v53, v57\n" \
    "v_add_f32 v61, v61, v65\n" \
    "v_mfma_f32_16x16x16_bf16 v[86:89], v[108:109], v[48:49], v[86:89]\n" \
    "v_add_f32 v74, v74, v80\n" \
    "v_add_f32 v75, v75, v81\n" \
    "ds_write_b64 v7, v[74:75] offset:512\n" \
    "LSpeven_%=:\n" \
    "v_add_f32 v50, v50, v58\n" \
    "v_add_f32 v51, v51, v59\n" \
    "v_add_f32 v52, v52, v60\n" \
    "v_add_f32 v53, v53, v61\n" \
    "v_cndmask_b32 v50, v113, v50, s[92:93]\n" \
    "v_cndmask_b32 v51, v113, v51, s[94:95]\n" \
    "v_cndmask_b32 v52, v113, v52, s[96:97]\n" \
    "v_cndmask_b32 v53, v113, v53, s[98:99]\n" \
    "v_add_f32 v82, v82, v88\n" \
    "v_add_f32 v83, v83, v89\n" \
    "ds_write_b64 v7, v[82:83] offset:1024\n" \
    "v_max3_f32 v54, v50, v51, v52\n" \
    "v_mfma_f32_16x16x16_bf16 v[18:21], v[116:117], v[116:117], 0\n" \
    "v_mfma_f32_16x16x16_bf16 v[18:21], v[116:117], v[116:117], 0\n" \
    "v_mfma_f32_16x16x16_bf16 v[18:21], v[116:117], v[116:117], 0\n" \
    "s_waitcnt lgkmcnt(0)\n" \
    "s_barrier\n" \
    "ds_read2st64_b32 v[90:91], v112 offset0:0 offset1:8\n" \
    "ds_read2st64_b32 v[92:93], v112 offset0:16 offset1:24\n" \
    "v_mfma_f32_16x16x16_bf16 v[18:21], v[116:117], v[116:117], 0\n" \
    "v_mfma_f32_16x16x16_bf16 v[18:21], v[116:117], v[116:117], 0\n" \
    "v_mfma_f32_16x16x16_bf16 v[18:21], v[116:117], v[116:117], 0\n" \
    "v_mfma_f32_16x16x16_bf16 v[18:21], v[116:117], v[116:117], 0\n" \
    "s_waitcnt lgkmcnt(0)\n" \
    "v_add_f32 v90, v90, v91\n" \
    "v_add_f32 v92, v92, v93\n" \
    "v_add_f32 v90, v90, v92\n" \
    "v_max3_f32 v54, v54, v53, v90\n" \
    "s_nop 1\n" \
    "v_max_f32_dpp v54, v54, v54 quad_perm:[1,0,3,2] row_mask:0xf bank_mask:0xf\n" \
    "s_nop 1\n" \
    "v_max_f32_dpp v54, v54, v54 quad_perm:[2,3,0,1] row_mask:0xf bank_mask:0xf\n" \
    "s_nop 1\n" \
    "v_max_f32_dpp v54, v54, v54 row_half_mirror row_mask:0xf bank_mask:0xf\n" \
    "v_sub_f32 v50, v50, v54\n" \
    "v_sub_f32 v51, v51, v54\n" \
    "v_sub_f32 v52, v52, v54\n" \
    "v_sub_f32 v53, v53, v54\n" \
    "v_sub_f32 v90, v90, v54\n" \
    "v_exp_f32 v50, v50\n" \
    "v_exp_f32 v51, v51\n" \
    "v_exp_f32 v52, v52\n" \
    "v_exp_f32 v53, v53\n" \
    "v_exp_f32 v90, v90\n" \
    "v_add_f32 v55, v50, v51\n" \
    "v_add_f32 v56, v52, v53\n" \
    "v_add_f32 v55, v55, v56\n" \
    "v_add_f32 v55, v55, v90\n" \
    "s_nop 1\n" \
    "v_add_f32_dpp v55, v55, v55 quad_perm:[1,0,3,2] row_mask:0xf bank_mask:0xf\n" \
    "s_nop 1\n" \
    "v_add_f32_dpp v55, v55, v55 quad_perm:[2,3,0,1] row_mask:0xf bank_mask:0xf\n" \
    "s_nop 1\n" \
    "v_add_f32_dpp v55, v55, v55 row_half_mirror row_mask:0xf bank_mask:0xf\n" \
    "v_rcp_f32 v55, v55\n" \
    "s_nop 0\n" \
    "v_mul_f32 v90, v90, v55\n" \
    "v_mul_f32 v50, v50, v55\n" \
    "v_mul_f32 v51, v51, v55\n" \
    "v_mul_f32 v52, v52, v55\n" \
    "v_mul_f32 v53, v53, v55\n" \
    "v_mov_b32_dpp v91, v90 quad_perm:[1,0,3,2] row_mask:0xf bank_mask:0xf\n" \
    "v_cvt_pk_bf16_f32 v50, |v50|, |v51|\n" \
    "v_cvt_pk_bf16_f32 v51, |v52|, |v53|\n" \
    "v_cvt_pk_bf16_f32 v90, |v90|, |v91|\n" \
    "buffer_store_dwordx2 v[50:51], v110, s[4:7], s24 offen sc1\n" \
    "buffer_store_dword v90, v115, s[4:7], s25 offen sc1\n" \
    "s_add_u32 s13, s13, 4\n" \
    "s_mov_b32 s10, s8\n" \
    "s_mov_b32 s8, s9\n" \
    "s_mov_b32 s9, s10\n" \
    "s_cmp_lt_u32 s13, 64\n" \
    "s_cbranch_scc1 LSturn_%=\n" \
    "s_endpgm\n" \
    "LSpoll_%=:\n" \
    "s_sub_u32 s17, s17, 1\n" \
    "s_cmp_lt_i32 s17, 0\n" \
    "s_cbranch_scc1 LSpolled_%=\n" \
    "s_sleep 1\n" \
    "buffer_load_dword v115, v114, s[4:7], s31 offen sc1\n" \
    "s_branch LSpoll1_%=\n" \
    "LSva_%=:\n" \
    "s_cmp_gt_u32 s1, 6\n" \
    "s_cselect_b32 s3, 517120, 0\n" \
    "s_lshl_b32 s2, s1, 10\n" \
    "s_add_u32 s2, s2, s3\n" \
    "s_add_u32 s2, s2, s0\n" \
    "s_add_u32 s1, s1, 1\n" \
    "s_cmp_eq_u32 s1, 14\n" \
    "s_cselect_b32 s1, 0, s1\n" \
    "buffer_load_dwordx4 a[56:59], v0, s[4:7], s2 offen\n" \
    "s_add_u32 s2, s2, 16384\n" \
    "buffer_load_dwordx4 a[64:67], v0, s[4:7], s2 offen\n" \
    "s_add_u32 s2, s2, 16384\n" \
    "s_xor_b32 s2, s2, 0x2000\n" \
    "buffer_load_dwordx4 a[72:75], v0, s[4:7], s2 offen\n" \
    "s_add_u32 s2, s2, 16384\n" \
    "s_xor_b32 s2, s2, 0x2000\n" \
    "buffer_load_dwordx4 a[80:83], v0, s[4:7], s2 offen\n" \
    "s_add_u32 s2, s2, 16384\n" \
    "buffer_load_dwordx4 a[88:91], v0, s[4:7], s2 offen\n" \
    "s_add_u32 s2, s2, 16384\n" \
    "s_xor_b32 s2, s2, 0x2000\n" \
    "buffer_load_dwordx4 a[96:99], v0, s[4:7], s2 offen\n" \
    "s_add_u32 s2, s2, 16384\n" \
    "s_xor_b32 s2, s2, 0x2000\n" \
    "buffer_load_dwordx4 a[104:107], v0, s[4:7], s2 offen\n" \
    "s_waitcnt vmcnt(14)\n" \
    "s_branch LSwa_%=\n" \
    "LSvb_%=:\n" \
    "s_cmp_gt_u32 s1, 6\n" \
    "s_cselect_b32 s3, 517120, 0\n" \
    "s_lshl_b32 s2, s1, 10\n" \
    "s_add_u32 s2, s2, s3\n" \
    "s_add_u32 s2, s2, s0\n" \
    "s_add_u32 s1, s1, 1\n" \
    "s_cmp_eq_u32 s1, 14\n" \
    "s_cselect_b32 s1, 0, s1\n" \
    "buffer_load_dwordx4 a[60:63], v0, s[4:7], s2 offen\n" \
    "s_add_u32 s2, s2, 16384\n" \
    "buffer_load_dwordx4 a[68:71], v0, s[4:7], s2 offen\n" \
    "s_add_u32 s2, s2, 16384\n" \
    "s_xor_b32 s2, s2, 0x2000\n" \
    "buffer_load_dwordx4 a[76:79], v0, s[4:7], s2 offen\n" \
    "s_add_u32 s2, s2, 16384\n" \
    "s_xor_b32 s2, s2, 0x2000\n" \
    "buffer_load_dwordx4 a[84:87], v0, s[4:7], s2 offen\n" \
    "s_add_u32 s2, s2, 16384\n" \
    "buffer_load_dwordx4 a[92:95], v0, s[4:7], s2 offen\n" \
    "s_add_u32 s2, s2, 16384\n" \
    "s_xor_b32 s2, s2, 0x2000\n" \
    "buffer_load_dwordx4 a[100:103], v0, s[4:7], s2 offen\n" \
    "s_add_u32 s2, s2, 16384\n" \
    "s_xor_b32 s2, s2, 0x2000\n" \
    "buffer_load_dwordx4 a[108:111], v0, s[4:7], s2 offen\n" \
    "s_waitcnt vmcnt(14)\n" \
    "s_branch LSwb_%=\n" \
    "LSvc_%=:\n" \
    "s_cmp_gt_u32 s1, 6\n" \
    "s_cselect_b32 s3, 517120, 0\n" \
    "s_lshl_b32 s2, s1, 10\n" \
    "s_add_u32 s2, s2, s3\n" \
    "s_add_u32 s2, s2, s0\n" \
    "s_add_u32 s1, s1, 1\n" \
    "s_cmp_eq_u32 s1, 14\n" \
    "s_cselect_b32 s1, 0, s1\n" \
    "buffer_load_dwordx4 a[112:115], v0, s[4:7], s2 offen\n" \
    "s_add_u32 s2, s2, 16384\n" \
    "buffer_load_dwordx4 a[120:123], v0, s[4:7], s2 offen\n" \
    "s_add_u32 s2, s2, 16384\n" \
    "s_xor_b32 s2, s2, 0x2000\n" \
    "buffer_load_dwordx4 a[128:131], v0, s[4:7], s2 offen\n" \
    "s_add_u32 s2, s2, 16384\n" \
    "s_xor_b32 s2, s2, 0x2000\n" \
    "buffer_load_dwordx4 a[136:139], v0, s[4:7], s2 offen\n" \
    "s_add_u32 s2, s2, 16384\n" \
    "buffer_load_dwordx4 a[144:147], v0, s[4:7], s2 offen\n" \
    "s_add_u32 s2, s2, 16384\n" \
    "s_xor_b32 s2, s2, 0x2000\n" \
    "buffer_load_dwordx4 a[152:155], v0, s[4:7], s2 offen\n" \
    "s_add_u32 s2, s2, 16384\n" \
    "s_xor_b32 s2, s2, 0x2000\n" \
    "buffer_load_dwordx4 a[160:163], v0, s[4:7], s2 offen\n" \
    "s_waitcnt vmcnt(14)\n" \
    "s_branch LSwc_%=\n" \
    "LSvd_%=:\n" \
    "s_cmp_gt_u32 s1, 6\n" \
    "s_cselect_b32 s3, 517120, 0\n" \
    "s_lshl_b32 s2, s1, 10\n" \
    "s_add_u32 s2, s2, s3\n" \
    "s_add_u32 s2, s2, s0\n" \
    "s_add_u32 s1, s1, 1\n" \
    "s_cmp_eq_u32 s1, 14\n" \
    "s_cselect_b32 s1, 0, s1\n" \
    "buffer_load_dwordx4 a[116:119], v0, s[4:7], s2 offen\n" \
    "s_add_u32 s2, s2, 16384\n" \
    "buffer_load_dwordx4 a[124:127], v0, s[4:7], s2 offen\n" \
    "s_add_u32 s2, s2, 16384\n" \
    "s_xor_b32 s2, s2, 0x2000\n" \
    "buffer_load_dwordx4 a[132:135], v0, s[4:7], s2 offen\n" \
    "s_add_u32 s2, s2, 16384\n" \
    "s_xor_b32 s2, s2, 0x2000\n" \
    "buffer_load_dwordx4 a[140:143], v0, s[4:7], s2 offen\n" \
    "s_add_u32 s2, s2, 16384\n" \
    "buffer_load_dwordx4 a[148:151], v0, s[4:7], s2 offen\n" \
    "s_add_u32 s2, s2, 16384\n" \
    "s_xor_b32 s2, s2, 0x2000\n" \
    "buffer_load_dwordx4 a[156:159], v0, s[4:7], s2 offen\n" \
    "s_add_u32 s2, s2, 16384\n" \
    "s_xor_b32 s2, s2, 0x2000\n" \
    "buffer_load_dwordx4 a[164:167], v0, s[4:7], s2 offen\n" \
    "s_waitcnt vmcnt(14)\n" \
    "s_branch LSwd_%=\n" \
    "LSve_%=:\n" \
    "s_cmp_gt_u32 s1, 6\n" \
    "s_cselect_b32 s3, 517120, 0\n" \
    "s_lshl_b32 s2, s1, 10\n" \
    "s_add_u32 s2, s2, s3\n" \
    "s_add_u32 s2, s2, s0\n" \
    "s_add_u32 s1, s1, 1\n" \
    "s_cmp_eq_u32 s1, 14\n" \
    "s_cselect_b32 s1, 0, s1\n" \
    "buffer_load_dwordx4 a[168:171], v0, s[4:7], s2 offen\n" \
    "s_add_u32 s2, s2, 16384\n" \
    "buffer_load_dwordx4 a[176:179], v0, s[4:7], s2 offen\n" \
    "s_add_u32 s2, s2, 16384\n" \
    "s_xor_b32 s2, s2, 0x2000\n" \
    "buffer_load_dwordx4 a[184:187], v0, s[4:7], s2 offen\n" \
    "s_add_u32 s2, s2, 16384\n" \
    "s_xor_b32 s2, s2, 0x2000\n" \
    "buffer_load_dwordx4 a[192:195], v0, s[4:7], s2 offen\n" \
    "s_add_u32 s2, s2, 16384\n" \
    "buffer_load_dwordx4 a[200:203], v0, s[4:7], s2 offen\n" \
    "s_add_u32 s2, s2, 16384\n" \
    "s_xor_b32 s2, s2, 0x2000\n" \
    "buffer_load_dwordx4 a[208:211], v0, s[4:7], s2 offen\n" \
    "s_add_u32 s2, s2, 16384\n" \
    "s_xor_b32 s2, s2, 0x2000\n" \
    "buffer_load_dwordx4 a[216:219], v0, s[4:7], s2 offen\n" \
    "s_waitcnt vmcnt(14)\n" \
    "s_branch LSwe_%=\n" \
    "LSvf_%=:\n" \
    "s_cmp_gt_u32 s1, 6\n" \
    "s_cselect_b32 s3, 517120, 0\n" \
    "s_lshl_b32 s2, s1, 10\n" \
    "s_add_u32 s2, s2, s3\n" \
    "s_add_u32 s2, s2, s0\n" \
    "s_add_u32 s1, s1, 1\n" \
    "s_cmp_eq_u32 s1, 14\n" \
    "s_cselect_b32 s1, 0, s1\n" \
    "buffer_load_dwordx4 a[172:175], v0, s[4:7], s2 offen\n" \
    "s_add_u32 s2, s2, 16384\n" \
    "buffer_load_dwordx4 a[180:183], v0, s[4:7], s2 offen\n" \
    "s_add_u32 s2, s2, 16384\n" \
    "s_xor_b32 s2, s2, 0x2000\n" \
    "buffer_load_dwordx4 a[188:191], v0, s[4:7], s2 offen\n" \
    "s_add_u32 s2, s2, 16384\n" \
    "s_xor_b32 s2, s2, 0x2000\n" \
    "buffer_load_dwordx4 a[196:199], v0, s[4:7], s2 offen\n" \
    "s_add_u32 s2, s2, 16384\n" \
    "buffer_load_dwordx4 a[204:207], v0, s[4:7], s2 offen\n" \
    "s_add_u32 s2, s2, 16384\n" \
    "s_xor_b32 s2, s2, 0x2000\n" \
    "buffer_load_dwordx4 a[212:215], v0, s[4:7], s2 offen\n" \
    "s_add_u32 s2, s2, 16384\n" \
    "s_xor_b32 s2, s2, 0x2000\n" \
    "buffer_load_dwordx4 a[220:223], v0, s[4:7], s2 offen\n" \
    "s_waitcnt vmcnt(14)\n" \
    "s_branch LSwf_%=\n" \
    "LSvg_%=:\n" \
    "s_cmp_gt_u32 s1, 6\n" \
    "s_cselect_b32 s3, 517120, 0\n" \
    "s_lshl_b32 s2, s1, 10\n" \
    "s_add_u32 s2, s2, s3\n" \
    "s_add_u32 s2, s2, s0\n" \
    "s_add_u32 s1, s1, 1\n" \
    "s_cmp_eq_u32 s1, 14\n" \
    "s_cselect_b32 s1, 0, s1\n" \
    "buffer_load_dwordx4 a[224:227], v0, s[4:7], s2 offen\n" \
    "s_add_u32 s2, s2, 16384\n" \
    "buffer_load_dwordx4 a[232:235], v0, s[4:7], s2 offen\n" \
    "s_add_u32 s2, s2, 16384\n" \
    "s_xor_b32 s2, s2, 0x2000\n" \
    "buffer_load_dwordx4 a[240:243], v0, s[4:7], s2 offen\n" \
    "s_add_u32 s2, s2, 16384\n" \
    "s_xor_b32 s2, s2, 0x2000\n" \
    "buffer_load_dwordx4 a[248:251], v0, s[4:7], s2 offen\n" \
    "s_add_u32 s2, s2, 16384\n" \
    "buffer_load_dwordx4 v[120:123], v0, s[4:7], s2 offen\n" \
    "s_add_u32 s2, s2, 16384\n" \
    "s_xor_b32 s2, s2, 0x2000\n" \
    "buffer_load_dwordx4 v[128:131], v0, s[4:7], s2 offen\n" \
    "s_add_u32 s2, s2, 16384\n" \
    "s_xor_b32 s2, s2, 0x2000\n" \
    "buffer_load_dwordx4 v[136:139], v0, s[4:7], s2 offen\n" \
    "s_waitcnt vmcnt(14)\n" \
    "s_branch LSwg_%=\n" \
    "LSvh_%=:\n" \
    "s_cmp_gt_u32 s1, 6\n" \
    "s_cselect_b32 s3, 517120, 0\n" \
    "s_lshl_b32 s2, s1, 10\n" \
    "s_add_u32 s2, s2, s3\n" \
    "s_add_u32 s2, s2, s0\n" \
    "s_add_u32 s1, s1, 1\n" \
    "s_cmp_eq_u32 s1, 14\n" \
    "s_cselect_b32 s1, 0, s1\n" \
    "buffer_load_dwordx4 a[228:231], v0, s[4:7], s2 offen\n" \
    "s_add_u32 s2, s2, 16384\n" \
    "buffer_load_dwordx4 a[236:239], v0, s[4:7], s2 offen\n" \
    "s_add_u32 s2, s2, 16384\n" \
    "s_xor_b32 s2, s2, 0x2000\n" \
    "buffer_load_dwordx4 a[244:247], v0, s[4:7], s2 offen\n" \
    "s_add_u32 s2, s2, 16384\n" \
    "s_xor_b32 s2, s2, 0x2000\n" \
    "buffer_load_dwordx4 a[252:255], v0, s[4:7], s2 offen\n" \
    "s_add_u32 s2, s2, 16384\n" \
    "buffer_load_dwordx4 v[124:127], v0, s[4:7], s2 offen\n" \
    "s_add_u32 s2, s2, 16384\n" \
    "s_xor_b32 s2, s2, 0x2000\n" \
    "buffer_load_dwordx4 v[132:135], v0, s[4:7], s2 offen\n" \
    "s_add_u32 s2, s2, 16384\n" \
    "s_xor_b32 s2, s2, 0x2000\n" \
    "buffer_load_dwordx4 v[140:143], v0, s[4:7], s2 offen\n" \
    "s_waitcnt vmcnt(14)\n" \
    "s_branch LSwh_%=\n" \
    "LSvi_%=:\n" \
    "s_cmp_gt_u32 s1, 6\n" \
    "s_cselect_b32 s3, 517120, 0\n" \
    "s_lshl_b32 s2, s1, 10\n" \
    "s_add_u32 s2, s2, s3\n" \
    "s_add_u32 s2, s2, s0\n" \
    "s_add_u32 s1, s1, 1\n" \
    "s_cmp_eq_u32 s1, 14\n" \
    "s_cselect_b32 s1, 0, s1\n" \
    "buffer_load_dwordx4 v[144:147], v0, s[4:7], s2 offen\n" \
    "s_add_u32 s2, s2, 16384\n" \
    "buffer_load_dwordx4 v[152:155], v0, s[4:7], s2 offen\n" \
    "s_add_u32 s2, s2, 16384\n" \
    "s_xor_b32 s2, s2, 0x2000\n" \
    "buffer_load_dwordx4 v[160:163], v0, s[4:7], s2 offen\n" \
    "s_add_u32 s2, s2, 16384\n" \
    "s_xor_b32 s2, s2, 0x2000\n" \
    "buffer_load_dwordx4 v[168:171], v0, s[4:7], s2 offen\n" \
    "s_add_u32 s2, s2, 16384\n" \
    "buffer_load_dwordx4 v[176:179], v0, s[4:7], s2 offen\n" \
    "s_add_u32 s2, s2, 16384\n" \
    "s_xor_b32 s2, s2, 0x2000\n" \
    "buffer_load_dwordx4 v[184:187], v0, s[4:7], s2 offen\n" \
    "s_add_u32 s2, s2, 16384\n" \
    "s_xor_b32 s2, s2, 0x2000\n" \
    "buffer_load_dwordx4 v[192:195], v0, s[4:7], s2 offen\n" \
    "s_waitcnt vmcnt(14)\n" \
    "s_branch LSwi_%=\n" \
    "LSvj_%=:\n" \
    "s_cmp_gt_u32 s1, 6\n" \
    "s_cselect_b32 s3, 517120, 0\n" \
    "s_lshl_b32 s2, s1, 10\n" \
    "s_add_u32 s2, s2, s3\n" \
    "s_add_u32 s2, s2, s0\n" \
    "s_add_u32 s1, s1, 1\n" \
    "s_cmp_eq_u32 s1, 14\n" \
    "s_cselect_b32 s1, 0, s1\n" \
    "buffer_load_dwordx4 v[148:151], v0, s[4:7], s2 offen\n" \
    "s_add_u32 s2, s2, 16384\n" \
    "buffer_load_dwordx4 v[156:159], v0, s[4:7], s2 offen\n" \
    "s_add_u32 s2, s2, 16384\n" \
    "s_xor_b32 s2, s2, 0x2000\n" \
    "buffer_load_dwordx4 v[164:167], v0, s[4:7], s2 offen\n" \
    "s_add_u32 s2, s2, 16384\n" \
    "s_xor_b32 s2, s2, 0x2000\n" \
    "buffer_load_dwordx4 v[172:175], v0, s[4:7], s2 offen\n" \
    "s_add_u32 s2, s2, 16384\n" \
    "buffer_load_dwordx4 v[180:183], v0, s[4:7], s2 offen\n" \
    "s_add_u32 s2, s2, 16384\n" \
    "s_xor_b32 s2, s2, 0x2000\n" \
    "buffer_load_dwordx4 v[188:191], v0, s[4:7], s2 offen\n" \
    "s_add_u32 s2, s2, 16384\n" \
    "s_xor_b32 s2, s2, 0x2000\n" \
    "buffer_load_dwordx4 v[196:199], v0, s[4:7], s2 offen\n" \
    "s_waitcnt vmcnt(14)\n" \
    "s_branch LSwj_%=\n" \
    "LSvk_%=:\n" \
    "s_cmp_gt_u32 s1, 6\n" \
    "s_cselect_b32 s3, 517120, 0\n" \
    "s_lshl_b32 s2, s1, 10\n" \
    "s_add_u32 s2, s2, s3\n" \
    "s_add_u32 s2, s2, s0\n" \
    "s_add_u32 s1, s1, 1\n" \
    "s_cmp_eq_u32 s1, 14\n" \
    "s_cselect_b32 s1, 0, s1\n" \
    "buffer_load_dwordx4 v[200:203], v0, s[4:7], s2 offen\n" \
    "s_add_u32 s2, s2, 16384\n" \
    "buffer_load_dwordx4 v[208:211], v0, s[4:7], s2 offen\n" \
    "s_add_u32 s2, s2, 16384\n" \
    "s_xor_b32 s2, s2, 0x2000\n" \
    "buffer_load_dwordx4 v[216:219], v0, s[4:7], s2 offen\n" \
    "s_add_u32 s2, s2, 16384\n" \
    "s_xor_b32 s2, s2, 0x2000\n" \
    "buffer_load_dwordx4 v[224:227], v0, s[4:7], s2 offen\n" \
    "s_add_u32 s2, s2, 16384\n" \
    "buffer_load_dwordx4 v[232:235], v0, s[4:7], s2 offen\n" \
    "s_add_u32 s2, s2, 16384\n" \
    "s_xor_b32 s2, s2, 0x2000\n" \
    "buffer_load_dwordx4 v[240:243], v0, s[4:7], s2 offen\n" \
    "s_add_u32 s2, s2, 16384\n" \
    "s_xor_b32 s2, s2, 0x2000\n" \
    "buffer_load_dwordx4 v[248:251], v0, s[4:7], s2 offen\n" \
    "s_waitcnt vmcnt(14)\n" \
    "s_branch LSwk_%=\n" \
    "LSvl_%=:\n" \
    "s_cmp_gt_u32 s1, 6\n" \
    "s_cselect_b32 s3, 517120, 0\n" \
    "s_lshl_b32 s2, s1, 10\n" \
    "s_add_u32 s2, s2, s3\n" \
    "s_add_u32 s2, s2, s0\n" \
    "s_add_u32 s1, s1, 1\n" \
    "s_cmp_eq_u32 s1, 14\n" \
    "s_cselect_b32 s1, 0, s1\n" \
    "buffer_load_dwordx4 v[204:207], v0, s[4:7], s2 offen\n" \
    "s_add_u32 s2, s2, 16384\n" \
    "buffer_load_dwordx4 v[212:215], v0, s[4:7], s2 offen\n" \
    "s_add_u32 s2, s2, 16384\n" \
    "s_xor_b32 s2, s2, 0x2000\n" \
    "buffer_load_dwordx4 v[220:223], v0, s[4:7], s2 offen\n" \
    "s_add_u32 s2, s2, 16384\n" \
    "s_xor_b32 s2, s2, 0x2000\n" \
    "buffer_load_dwordx4 v[228:231], v0, s[4:7], s2 offen\n" \
    "s_add_u32 s2, s2, 16384\n" \
    "buffer_load_dwordx4 v[236:239], v0, s[4:7], s2 offen\n" \
    "s_add_u32 s2, s2, 16384\n" \
    "s_xor_b32 s2, s2, 0x2000\n" \
    "buffer_load_dwordx4 v[244:247], v0, s[4:7], s2 offen\n" \
    "s_add_u32 s2, s2, 16384\n" \
    "s_xor_b32 s2, s2, 0x2000\n" \
    "buffer_load_dwordx4 v[252:255], v0, s[4:7], s2 offen\n" \
    "s_waitcnt vmcnt(14)\n" \
    "s_branch LSwl_%=\n" \
    "LSvm_%=:\n" \
    "s_waitcnt vmcnt(7)\n" \
    "s_branch LSwm_%=\n" \
    "LSvn_%=:\n" \
    "LSend_%=:\n" \
    "s_waitcnt vmcnt(0)\n" \
    "s_branch LSwn_%=\n"
#define O_ASM \
    "s_mov_b32 s80, %8\n" \
    "s_mov_b32 s81, %9\n" \
    "s_mov_b32 s82, %10\n" \
    "s_mov_b32 s83, %11\n" \
    "s_mov_b64 s[4:5], %0\n" \
    "s_mov_b32 s6, %7\n" \
    "s_mov_b32 s7, 0x20000\n" \
    "s_mov_b64 s[8:9], %1\n" \
    "s_mov_b32 s10, 0x1c0000\n" \
    "s_mov_b32 s11, 0x20000\n" \
    "s_mov_b32 s12, %2\n" \
    "s_mov_b32 s13, %3\n" \
    "s_mov_b32 s14, %4\n" \
    "s_mov_b32 s15, %5\n" \
    "s_mov_b32 s16, %6\n" \
    "s_and_b32 s5, s5, 0xffff\n" \
    "s_and_b32 s9, s9, 0xffff\n" \
    "s_mov_b32 s17, 300000\n" \
    "s_mov_b32 s19, 0\n" \
    "s_mov_b32 s76, 0\n" \
    "s_mov_b32 s20, 28672\n" \
    "v_mbcnt_lo_u32_b32 v0, -1, 0\n" \
    "v_mbcnt_hi_u32_b32 v0, -1, v0\n" \
    "v_lshlrev_b32 v1, 4, v0\n" \
    "v_and_b32 v152, 15, v0\n" \
    "s_getpc_b64 s[0:1]\n" \
    "LOpc_%=:\n" \
    "s_lshl_b32 s2, s14, 12\n" \
    "v_lshl_add_u32 v154, v0, 6, s2\n" \
    "v_min_u32 v154, LOend_%=-LOpc_%=, v154\n" \
    "s_mov_b32 m0, 99328\n" \
    "s_nop 0\n" \
    "global_load_lds_dword v154, s[0:1]\n" \
    "s_add_u32 s26, s13, 0\n" \
    "s_min_u32 s26, s26, 63\n" \
    "s_lshl_b32 s26, s26, 2\n" \
    "s_add_u32 s26, s26, s14\n" \
    "s_lshl_b32 s26, s26, 14\n" \
    "s_lshr_b32 s27, s26, 8\n" \
    "s_and_b32 s27, s27, 0x2000\n" \
    "s_add_u32 s26, s26, s27\n" \
    "s_add_u32 s26, s26, 44040192\n" \
    "s_mul_i32 s27, s14, 7168\n" \
    "s_add_u32 m0, s27, s19\n" \
    "s_nop 0\n" \
    "buffer_load_dwordx4 v1, s[4:7], s26 offen offset:0 lds\n" \
    "buffer_load_dwordx4 v1, s[4:7], s26 offen offset:1024 lds\n" \
    "buffer_load_dwordx4 v1, s[4:7], s26 offen offset:2048 lds\n" \
    "buffer_load_dwordx4 v1, s[4:7], s26 offen offset:3072 lds\n" \
    "s_add_u32 m0, m0, 4096\n" \
    "s_add_u32 s26, s26, 4096\n" \
    "buffer_load_dwordx4 v1, s[4:7], s26 offen offset:0 lds\n" \
    "buffer_load_dwordx4 v1, s[4:7], s26 offen offset:1024 lds\n" \
    "buffer_load_dwordx4 v1, s[4:7], s26 offen offset:2048 lds\n" \
    "s_lshl_b32 s0, s14, 2\n" \
    "s_add_u32 s0, s0, 16\n" \
    "s_mul_i32 s0, s0, 28\n" \
    "s_add_u32 s0, s0, s12\n" \
    "s_mul_i32 s0, s0, 14336\n" \
    "s_add_u32 s0, s0, 4194304\n" \
    "s_lshl_b32 s1, s14, 1\n" \
    "s_lshl_b32 s2, s1, 11\n" \
    "s_add_u32 s2, s2, s0\n" \
    "s_add_u32 s1, s1, 1\n" \
    "s_cmp_eq_u32 s1, 7\n" \
    "s_cselect_b32 s1, 0, s1\n" \
    "buffer_load_dwordx4 a[0:3], v1, s[4:7], s2 offen offset:0\n" \
    "buffer_load_dwordx4 a[4:7], v1, s[4:7], s2 offen offset:1024\n" \
    "s_add_u32 s2, s2, 401408\n" \
    "buffer_load_dwordx4 a[8:11], v1, s[4:7], s2 offen offset:0\n" \
    "buffer_load_dwordx4 a[12:15], v1, s[4:7], s2 offen offset:1024\n" \
    "s_add_u32 s2, s2, 401408\n" \
    "buffer_load_dwordx4 a[16:19], v1, s[4:7], s2 offen offset:0\n" \
    "buffer_load_dwordx4 a[20:23], v1, s[4:7], s2 offen offset:1024\n" \
    "s_add_u32 s2, s2, 401408\n" \
    "buffer_load_dwordx4 a[24:27], v1, s[4:7], s2 offen offset:0\n" \
    "buffer_load_dwordx4 a[28:31], v1, s[4:7], s2 offen offset:1024\n" \
    "s_lshl_b32 s2, s1, 11\n" \
    "s_add_u32 s2, s2, s0\n" \
    "s_add_u32 s1, s1, 1\n" \
    "s_cmp_eq_u32 s1, 7\n" \
    "s_cselect_b32 s1, 0, s1\n" \
    "buffer_load_dwordx4 a[32:35], v1, s[4:7], s2 offen offset:0\n" \
    "buffer_load_dwordx4 a[36:39], v1, s[4:7], s2 offen offset:1024\n" \
    "s_add_u32 s2, s2, 401408\n" \
    "buffer_load_dwordx4 a[40:43], v1, s[4:7], s2 offen offset:0\n" \
    "buffer_load_dwordx4 a[44:47], v1, s[4:7], s2 offen offset:1024\n" \
    "s_add_u32 s2, s2, 401408\n" \
    "buffer_load_dwordx4 a[48:51], v1, s[4:7], s2 offen offset:0\n" \
    "buffer_load_dwordx4 a[52:55], v1, s[4:7], s2 offen offset:1024\n" \
    "s_add_u32 s2, s2, 401408\n" \
    "buffer_load_dwordx4 a[56:59], v1, s[4:7], s2 offen offset:0\n" \
    "buffer_load_dwordx4 a[60:63], v1, s[4:7], s2 offen offset:1024\n" \
    "s_lshl_b32 s25, s14, 1\n" \
    "s_lshl_b32 s26, s25, 11\n" \
    "v_add_u32 v12, s26, v1\n" \
    "s_lshl_b32 s27, s25, 10\n" \
    "s_add_u32 s27, s27, 57344\n" \
    "v_add_u32 v20, s27, v1\n" \
    "s_add_u32 s25, s25, 1\n" \
    "s_cmp_eq_u32 s25, 7\n" \
    "s_cselect_b32 s25, 0, s25\n" \
    "s_lshl_b32 s26, s25, 11\n" \
    "v_add_u32 v13, s26, v1\n" \
    "s_lshl_b32 s27, s25, 10\n" \
    "s_add_u32 s27, s27, 57344\n" \
    "v_add_u32 v21, s27, v1\n" \
    "s_add_u32 s25, s25, 1\n" \
    "s_cmp_eq_u32 s25, 7\n" \
    "s_cselect_b32 s25, 0, s25\n" \
    "s_lshl_b32 s26, s25, 11\n" \
    "v_add_u32 v14, s26, v1\n" \
    "s_lshl_b32 s27, s25, 10\n" \
    "s_add_u32 s27, s27, 57344\n" \
    "v_add_u32 v22, s27, v1\n" \
    "s_add_u32 s25, s25, 1\n" \
    "s_cmp_eq_u32 s25, 7\n" \
    "s_cselect_b32 s25, 0, s25\n" \
    "s_lshl_b32 s26, s25, 11\n" \
    "v_add_u32 v15, s26, v1\n" \
    "s_lshl_b32 s27, s25, 10\n" \
    "s_add_u32 s27, s27, 57344\n" \
    "v_add_u32 v23, s27, v1\n" \
    "s_add_u32 s25, s25, 1\n" \
    "s_cmp_eq_u32 s25, 7\n" \
    "s_cselect_b32 s25, 0, s25\n" \
    "s_lshl_b32 s26, s25, 11\n" \
    "v_add_u32 v16, s26, v1\n" \
    "s_lshl_b32 s27, s25, 10\n" \
    "s_add_u32 s27, s27, 57344\n" \
    "v_add_u32 v24, s27, v1\n" \
    "s_add_u32 s25, s25, 1\n" \
    "s_cmp_eq_u32 s25, 7\n" \
    "s_cselect_b32 s25, 0, s25\n" \
    "s_lshl_b32 s26, s25, 11\n" \
    "v_add_u32 v17, s26, v1\n" \
    "s_lshl_b32 s27, s25, 10\n" \
    "s_add_u32 s27, s27, 57344\n" \
    "v_add_u32 v25, s27, v1\n" \
    "s_add_u32 s25, s25, 1\n" \
    "s_cmp_eq_u32 s25, 7\n" \
    "s_cselect_b32 s25, 0, s25\n" \
    "s_lshl_b32 s26, s25, 11\n" \
    "v_add_u32 v18, s26, v1\n" \
    "s_lshl_b32 s27, s25, 10\n" \
    "s_add_u32 s27, s27, 57344\n" \
    "v_add_u32 v26, s27, v1\n" \
    "s_add_u32 s25, s25, 1\n" \
    "s_cmp_eq_u32 s25, 7\n" \
    "s_cselect_b32 s25, 0, s25\n" \
    "v_lshrrev_b32 v153, 4, v0\n" \
    "v_and_b32 v154, 1, v0\n" \
    "v_cmp_eq_u32 vcc, 1, v154\n" \
    "v_mov_b32 v2, 0x4444\n" \
    "v_mov_b32 v155, 0xeeee\n" \
    "v_cndmask_b32 v2, v2, v155, vcc\n" \
    "v_mov_b32 v5, 0x05040100\n" \
    "v_mov_b32 v155, 0x03020706\n" \
    "v_cndmask_b32 v5, v5, v155, vcc\n" \
    "v_add_u32 v3, 64512, v1\n" \
    "v_and_b32 v155, 14, v0\n" \
    "s_lshl_b32 s24, s14, 11\n" \
    "v_and_b32 v155, 8, v0\n" \
    "v_lshl_add_u32 v7, v155, 6, s24\n" \
    "v_cmp_eq_u32 vcc, 8, v155\n" \
    "v_mov_b32 v6, 0x4444\n" \
    "v_mov_b32 v155, 0xeeee\n" \
    "v_cndmask_b32 v6, v6, v155, vcc\n" \
    "v_and_b32 v155, 7, v0\n" \
    "v_lshl_add_u32 v7, v155, 5, v7\n" \
    "v_and_b32 v155, 32, v0\n" \
    "v_lshl_add_u32 v7, v155, 3, v7\n" \
    "v_and_b32 v155, 16, v0\n" \
    "v_add_u32 v7, v7, v155\n" \
    "s_lshl_b32 s25, s12, 8\n" \
    "v_lshl_add_u32 v4, v153, 6, s25\n" \
    "v_lshl_add_u32 v4, v154, 5, v4\n" \
    "v_and_b32 v155, 2, v0\n" \
    "v_lshl_add_u32 v4, v155, 3, v4\n" \
    "v_and_b32 v155, 12, v0\n" \
    "v_add_u32 v4, v4, v155\n" \
    "v_lshlrev_b32 v8, 7, v152\n" \
    "v_lshl_add_u32 v8, v153, 28, v8\n" \
    "s_lshl_b32 s25, s14, 26\n" \
    "v_or_b32 v19, s25, v8\n" \
    "s_xor_b32 s25, s25, 0x4000000\n" \
    "v_or_b32 v27, s25, v8\n" \
    "s_add_u32 s24, s24, 64512\n" \
    "v_add_u32 v148, s24, v1\n" \
    "v_add_u32 v149, 1024, v148\n" \
    "v_lshl_add_u32 v9, v152, 4, s24\n" \
    "v_lshrrev_b32 v155, 1, v153\n" \
    "v_lshl_add_u32 v9, v155, 8, v9\n" \
    "v_and_b32 v155, 1, v153\n" \
    "v_lshl_add_u32 v9, v155, 3, v9\n" \
    "v_subrev_u32 v11, 57344, v21\n" \
    "v_subrev_u32 v10, 57344, v20\n" \
    "s_waitcnt vmcnt(16)\n" \
    "s_barrier\n" \
    "LOturn_%=:\n" \
    "ds_read_b128 v[28:31], v12 offset:0\n" \
    "ds_read_b128 v[32:35], v12 offset:14336\n" \
    "s_add_u32 s19, s19, s20\n" \
    "s_cmp_lt_u32 s13, 2\n" \
    "s_cselect_b64 vcc, -1, 0\n" \
    "s_cbranch_vccnz LOva_%=\n" \
    "LOwa_%=:\n" \
    "s_waitcnt lgkmcnt(0)\n" \
    "v_mfma_f32_16x16x32_bf16 v[44:47], v[28:31], a[0:3], 0\n" \
    "ds_read_b128 v[36:39], v12 offset:1024\n" \
    "ds_read_b128 v[40:43], v12 offset:15360\n" \
    "v_mfma_f32_16x16x32_bf16 v[48:51], v[32:35], a[0:3], 0\n" \
    "s_lshr_b32 s24, s13, 3\n" \
    "s_and_b32 s24, s24, 2\n" \
    "v_mfma_f32_16x16x32_bf16 v[52:55], v[28:31], a[8:11], 0\n" \
    "s_lshl_b32 s25, s13, 15\n" \
    "s_xor_b32 s22, s24, s15\n" \
    "v_mfma_f32_16x16x32_bf16 v[56:59], v[32:35], a[8:11], 0\n" \
    "s_xor_b32 s22, s22, 2\n" \
    "s_lshl_b32 s22, s22, 13\n" \
    "v_mfma_f32_16x16x32_bf16 v[60:63], v[28:31], a[16:19], 0\n" \
    "s_add_u32 s22, s22, s25\n" \
    "s_xor_b32 s35, s24, s16\n" \
    "v_mfma_f32_16x16x32_bf16 v[64:67], v[32:35], a[16:19], 0\n" \
    "s_lshl_b32 s35, s35, 13\n" \
    "s_add_u32 s35, s35, s25\n" \
    "v_mfma_f32_16x16x32_bf16 v[68:71], v[28:31], a[24:27], 0\n" \
    "s_add_u32 s35, s35, 2097152\n" \
    "s_xor_b32 s23, s35, 0x4000\n" \
    "v_mfma_f32_16x16x32_bf16 v[72:75], v[32:35], a[24:27], 0\n" \
    "s_waitcnt lgkmcnt(0)\n" \
    "v_mfma_f32_16x16x32_bf16 v[44:47], v[36:39], a[4:7], v[44:47]\n" \
    "ds_read_b128 v[28:31], v13 offset:0\n" \
    "ds_read_b128 v[32:35], v13 offset:14336\n" \
    "v_mfma_f32_16x16x32_bf16 v[48:51], v[40:43], a[4:7], v[48:51]\n" \
    "s_cmp_eq_u32 s13, 0\n" \
    "s_cselect_b32 s21, 24203264, s35\n" \
    "v_mfma_f32_16x16x32_bf16 v[52:55], v[36:39], a[12:15], v[52:55]\n" \
    "s_add_u32 s25, s13, 1\n" \
    "s_lshr_b32 s24, s25, 3\n" \
    "v_mfma_f32_16x16x32_bf16 v[56:59], v[40:43], a[12:15], v[56:59]\n" \
    "s_and_b32 s24, s24, 2\n" \
    "s_lshl_b32 s25, s25, 15\n" \
    "v_mfma_f32_16x16x32_bf16 v[60:63], v[36:39], a[20:23], v[60:63]\n" \
    "s_xor_b32 s38, s24, s15\n" \
    "s_lshl_b32 s38, s38, 13\n" \
    "v_mfma_f32_16x16x32_bf16 v[64:67], v[40:43], a[20:23], v[64:67]\n" \
    "s_add_u32 s38, s38, s25\n" \
    "s_xor_b32 s39, s24, s16\n" \
    "v_mfma_f32_16x16x32_bf16 v[68:71], v[36:39], a[28:31], v[68:71]\n" \
    "s_lshl_b32 s39, s39, 13\n" \
    "s_add_u32 s39, s39, s25\n" \
    "v_mfma_f32_16x16x32_bf16 v[72:75], v[40:43], a[28:31], v[72:75]\n" \
    "s_cbranch_vccnz LOvb_%=\n" \
    "LOwb_%=:\n" \
    "s_waitcnt lgkmcnt(0)\n" \
    "v_mfma_f32_16x16x32_bf16 v[44:47], v[28:31], a[32:35], v[44:47]\n" \
    "ds_read_b128 v[36:39], v13 offset:1024\n" \
    "ds_read_b128 v[40:43], v13 offset:15360\n" \
    "v_mfma_f32_16x16x32_bf16 v[48:51], v[32:35], a[32:35], v[48:51]\n" \
    "s_add_u32 s39, s39, 2097152\n" \
    "s_sub_u32 s25, s13, 1\n" \
    "v_mfma_f32_16x16x32_bf16 v[52:55], v[28:31], a[40:43], v[52:55]\n" \
    "s_max_i32 s25, s25, 0\n" \
    "s_lshr_b32 s24, s25, 3\n" \
    "v_mfma_f32_16x16x32_bf16 v[56:59], v[32:35], a[40:43], v[56:59]\n" \
    "s_and_b32 s24, s24, 2\n" \
    "s_xor_b32 s24, s24, s15\n" \
    "v_mfma_f32_16x16x32_bf16 v[60:63], v[28:31], a[48:51], v[60:63]\n" \
    "s_xor_b32 s24, s24, 2\n" \
    "s_lshl_b32 s24, s24, 13\n" \
    "v_mfma_f32_16x16x32_bf16 v[64:67], v[32:35], a[48:51], v[64:67]\n" \
    "s_lshl_b32 s25, s25, 15\n" \
    "s_add_u32 s10, s24, s25\n" \
    "v_mfma_f32_16x16x32_bf16 v[68:71], v[28:31], a[56:59], v[68:71]\n" \
    "s_add_u32 s10, s10, 6144\n" \
    "v_mov_b32 v120, 0\n" \
    "v_mfma_f32_16x16x32_bf16 v[72:75], v[32:35], a[56:59], v[72:75]\n" \
    "v_add_u32 v12, s20, v12\n" \
    "s_waitcnt lgkmcnt(0)\n" \
    "v_mfma_f32_16x16x32_bf16 v[44:47], v[36:39], a[36:39], v[44:47]\n" \
    "ds_read_b128 v[28:31], v14 offset:0\n" \
    "ds_read_b128 v[32:35], v14 offset:14336\n" \
    "v_mfma_f32_16x16x32_bf16 v[48:51], v[40:43], a[36:39], v[48:51]\n" \
    "v_mov_b32 v121, 0\n" \
    "v_mov_b32 v122, 0\n" \
    "v_mfma_f32_16x16x32_bf16 v[52:55], v[36:39], a[44:47], v[52:55]\n" \
    "v_mov_b32 v123, 0\n" \
    "v_mov_b32 v124, 0\n" \
    "v_mfma_f32_16x16x32_bf16 v[56:59], v[40:43], a[44:47], v[56:59]\n" \
    "v_mov_b32 v125, 0\n" \
    "v_mov_b32 v126, 0\n" \
    "v_mfma_f32_16x16x32_bf16 v[60:63], v[36:39], a[52:55], v[60:63]\n" \
    "v_mov_b32 v127, 0\n" \
    "v_mov_b32 v128, 0\n" \
    "v_mfma_f32_16x16x32_bf16 v[64:67], v[40:43], a[52:55], v[64:67]\n" \
    "v_mov_b32 v129, 0\n" \
    "v_mov_b32 v130, 0\n" \
    "v_mfma_f32_16x16x32_bf16 v[68:71], v[36:39], a[60:63], v[68:71]\n" \
    "v_mov_b32 v131, 0\n" \
    "v_mov_b32 v132, 0\n" \
    "v_mfma_f32_16x16x32_bf16 v[72:75], v[40:43], a[60:63], v[72:75]\n" \
    "s_cbranch_vccnz LOvc_%=\n" \
    "LOwc_%=:\n" \
    "s_waitcnt lgkmcnt(0)\n" \
    "v_mfma_f32_16x16x32_bf16 v[44:47], v[28:31], a[64:67], v[44:47]\n" \
    "ds_read_b128 v[36:39], v14 offset:1024\n" \
    "ds_read_b128 v[40:43], v14 offset:15360\n" \
    "v_mfma_f32_16x16x32_bf16 v[48:51], v[32:35], a[64:67], v[48:51]\n" \
    "v_mov_b32 v133, 0\n" \
    "v_mov_b32 v134, 0\n" \
    "v_mfma_f32_16x16x32_bf16 v[52:55], v[28:31], a[72:75], v[52:55]\n" \
    "v_mov_b32 v135, 0\n" \
    "v_mov_b32 v136, 0\n" \
    "v_mfma_f32_16x16x32_bf16 v[56:59], v[32:35], a[72:75], v[56:59]\n" \
    "v_mov_b32 v137, 0\n" \
    "v_mov_b32 v138, 0\n" \
    "v_mfma_f32_16x16x32_bf16 v[60:63], v[28:31], a[80:83], v[60:63]\n" \
    "v_mov_b32 v139, 0\n" \
    "v_mov_b32 v140, 0\n" \
    "v_mfma_f32_16x16x32_bf16 v[64:67], v[32:35], a[80:83], v[64:67]\n" \
    "v_mov_b32 v141, 0\n" \
    "v_mov_b32 v142, 0\n" \
    "v_mfma_f32_16x16x32_bf16 v[68:71], v[28:31], a[88:91], v[68:71]\n" \
    "v_mov_b32 v143, 0\n" \
    "s_add_u32 s26, s13, 2\n" \
    "v_mfma_f32_16x16x32_bf16 v[72:75], v[32:35], a[88:91], v[72:75]\n" \
    "v_add_u32 v13, s20, v13\n" \
    "s_waitcnt lgkmcnt(0)\n" \
    "v_mfma_f32_16x16x32_bf16 v[44:47], v[36:39], a[68:71], v[44:47]\n" \
    "ds_read_b128 v[28:31], v15 offset:0\n" \
    "ds_read_b128 v[32:35], v15 offset:14336\n" \
    "v_mfma_f32_16x16x32_bf16 v[48:51], v[40:43], a[68:71], v[48:51]\n" \
    "s_min_u32 s26, s26, 63\n" \
    "s_lshl_b32 s26, s26, 2\n" \
    "v_mfma_f32_16x16x32_bf16 v[52:55], v[36:39], a[76:79], v[52:55]\n" \
    "s_add_u32 s26, s26, s14\n" \
    "s_lshl_b32 s26, s26, 14\n" \
    "v_mfma_f32_16x16x32_bf16 v[56:59], v[40:43], a[76:79], v[56:59]\n" \
    "s_lshr_b32 s27, s26, 8\n" \
    "s_and_b32 s27, s27, 0x2000\n" \
    "v_mfma_f32_16x16x32_bf16 v[60:63], v[36:39], a[84:87], v[60:63]\n" \
    "s_add_u32 s26, s26, s27\n" \
    "s_add_u32 s26, s26, 44040192\n" \
    "v_mfma_f32_16x16x32_bf16 v[64:67], v[40:43], a[84:87], v[64:67]\n" \
    "s_mul_i32 s27, s14, 7168\n" \
    "s_add_u32 m0, s27, s19\n" \
    "v_mfma_f32_16x16x32_bf16 v[68:71], v[36:39], a[92:95], v[68:71]\n" \
    "s_nop 0\n" \
    "v_mfma_f32_16x16x32_bf16 v[72:75], v[40:43], a[92:95], v[72:75]\n" \
    "s_cbranch_vccnz LOvd_%=\n" \
    "LOwd_%=:\n" \
    "s_waitcnt lgkmcnt(0)\n" \
    "v_mfma_f32_16x16x32_bf16 v[44:47], v[28:31], a[96:99], v[44:47]\n" \
    "ds_read_b128 v[36:39], v15 offset:1024\n" \
    "ds_read_b128 v[40:43], v15 offset:15360\n" \
    "v_mfma_f32_16x16x32_bf16 v[48:51], v[32:35], a[96:99], v[48:51]\n" \
    "v_mfma_f32_16x16x32_bf16 v[52:55], v[28:31], a[104:107], v[52:55]\n" \
    "buffer_load_dwordx4 v1, s[4:7], s26 offen offset:0 lds\n" \
    "buffer_load_dwordx4 v1, s[4:7], s26 offen offset:1024 lds\n" \
    "v_mfma_f32_16x16x32_bf16 v[56:59], v[32:35], a[104:107], v[56:59]\n" \
    "v_add_u32 v14, s20, v14\n" \
    "v_mfma_f32_16x16x32_bf16 v[60:63], v[28:31], a[112:115], v[60:63]\n" \
    "v_mfma_f32_16x16x32_bf16 v[64:67], v[32:35], a[112:115], v[64:67]\n" \
    "v_mfma_f32_16x16x32_bf16 v[68:71], v[28:31], a[120:123], v[68:71]\n" \
    "v_mfma_f32_16x16x32_bf16 v[72:75], v[32:35], a[120:123], v[72:75]\n" \
    "s_waitcnt lgkmcnt(0)\n" \
    "v_mfma_f32_16x16x32_bf16 v[44:47], v[36:39], a[100:103], v[44:47]\n" \
    "ds_read_b128 v[28:31], v16 offset:0\n" \
    "ds_read_b128 v[32:35], v16 offset:14336\n" \
    "v_mfma_f32_16x16x32_bf16 v[48:51], v[40:43], a[100:103], v[48:51]\n" \
    "v_mfma_f32_16x16x32_bf16 v[52:55], v[36:39], a[108:111], v[52:55]\n" \
    "buffer_load_dwordx4 v1, s[4:7], s26 offen offset:2048 lds\n" \
    "buffer_load_dwordx4 v1, s[4:7], s26 offen offset:3072 lds\n" \
    "v_mfma_f32_16x16x32_bf16 v[56:59], v[40:43], a[108:111], v[56:59]\n" \
    "v_mfma_f32_16x16x32_bf16 v[60:63], v[36:39], a[116:119], v[60:63]\n" \
    "v_mfma_f32_16x16x32_bf16 v[64:67], v[40:43], a[116:119], v[64:67]\n" \
    "v_mfma_f32_16x16x32_bf16 v[68:71], v[36:39], a[124:127], v[68:71]\n" \
    "v_mfma_f32_16x16x32_bf16 v[72:75], v[40:43], a[124:127], v[72:75]\n" \
    "s_cbranch_vccnz LOve_%=\n" \
    "LOwe_%=:\n" \
    "s_waitcnt lgkmcnt(0)\n" \
    "v_mfma_f32_16x16x32_bf16 v[44:47], v[28:31], a[128:131], v[44:47]\n" \
    "ds_read_b128 v[36:39], v16 offset:1024\n" \
    "ds_read_b128 v[40:43], v16 offset:15360\n" \
    "v_mfma_f32_16x16x32_bf16 v[48:51], v[32:35], a[128:131], v[48:51]\n" \
    "v_mfma_f32_16x16x32_bf16 v[52:55], v[28:31], a[136:139], v[52:55]\n" \
    "s_add_u32 m0, m0, 4096\n" \
    "s_add_u32 s26, s26, 4096\n" \
    "buffer_load_dwordx4 v1, s[4:7], s26 offen offset:0 lds\n" \
    "v_mfma_f32_16x16x32_bf16 v[56:59], v[32:35], a[136:139], v[56:59]\n" \
    "buffer_load_dwordx4 v1, s[4:7], s26 offen offset:1024 lds\n" \
    "v_add_u32 v15, s20, v15\n" \
    "v_mfma_f32_16x16x32_bf16 v[60:63], v[28:31], a[144:147], v[60:63]\n" \
    "v_mfma_f32_16x16x32_bf16 v[64:67], v[32:35], a[144:147], v[64:67]\n" \
    "v_mfma_f32_16x16x32_bf16 v[68:71], v[28:31], a[152:155], v[68:71]\n" \
    "v_mfma_f32_16x16x32_bf16 v[72:75], v[32:35], a[152:155], v[72:75]\n" \
    "s_waitcnt lgkmcnt(0)\n" \
    "v_mfma_f32_16x16x32_bf16 v[44:47], v[36:39], a[132:135], v[44:47]\n" \
    "ds_read_b128 v[28:31], v17 offset:0\n" \
    "ds_read_b128 v[32:35], v17 offset:14336\n" \
    "v_mfma_f32_16x16x32_bf16 v[48:51], v[40:43], a[132:135], v[48:51]\n" \
    "v_mfma_f32_16x16x32_bf16 v[52:55], v[36:39], a[140:143], v[52:55]\n" \
    "buffer_load_dwordx4 v1, s[4:7], s26 offen offset:2048 lds\n" \
    "v_mfma_f32_16x16x32_bf16 v[56:59], v[40:43], a[140:143], v[56:59]\n" \
    "v_mfma_f32_16x16x32_bf16 v[60:63], v[36:39], a[148:151], v[60:63]\n" \
    "v_mfma_f32_16x16x32_bf16 v[64:67], v[40:43], a[148:151], v[64:67]\n" \
    "v_mfma_f32_16x16x32_bf16 v[68:71], v[36:39], a[156:159], v[68:71]\n" \
    "v_mfma_f32_16x16x32_bf16 v[72:75], v[40:43], a[156:159], v[72:75]\n" \
    "s_cbranch_vccnz LOvf_%=\n" \
    "LOwf_%=:\n" \
    "s_waitcnt lgkmcnt(0)\n" \
    "v_mfma_f32_16x16x32_bf16 v[44:47], v[28:31], a[160:163], v[44:47]\n" \
    "ds_read_b128 v[36:39], v17 offset:1024\n" \
    "ds_read_b128 v[40:43], v17 offset:15360\n" \
    "v_mfma_f32_16x16x32_bf16 v[48:51], v[32:35], a[160:163], v[48:51]\n" \
    "v_add_u32 v16, s20, v16\n" \
    "v_mfma_f32_16x16x32_bf16 v[52:55], v[28:31], a[168:171], v[52:55]\n" \
    "v_mfma_f32_16x16x32_bf16 v[56:59], v[32:35], a[168:171], v[56:59]\n" \
    "v_mfma_f32_16x16x32_bf16 v[60:63], v[28:31], a[176:179], v[60:63]\n" \
    "v_mfma_f32_16x16x32_bf16 v[64:67], v[32:35], a[176:179], v[64:67]\n" \
    "v_mfma_f32_16x16x32_bf16 v[68:71], v[28:31], a[184:187], v[68:71]\n" \
    "v_mfma_f32_16x16x32_bf16 v[72:75], v[32:35], a[184:187], v[72:75]\n" \
    "s_waitcnt lgkmcnt(0)\n" \
    "v_mfma_f32_16x16x32_bf16 v[44:47], v[36:39], a[164:167], v[44:47]\n" \
    "ds_read_b128 v[28:31], v18 offset:0\n" \
    "ds_read_b128 v[32:35], v18 offset:14336\n" \
    "v_mfma_f32_16x16x32_bf16 v[48:51], v[40:43], a[164:167], v[48:51]\n" \
    "buffer_load_dword v153, v27, s[4:7], s10 offen sc1\n" \
    "v_mfma_f32_16x16x32_bf16 v[52:55], v[36:39], a[172:175], v[52:55]\n" \
    "v_mfma_f32_16x16x32_bf16 v[56:59], v[40:43], a[172:175], v[56:59]\n" \
    "v_mfma_f32_16x16x32_bf16 v[60:63], v[36:39], a[180:183], v[60:63]\n" \
    "v_mfma_f32_16x16x32_bf16 v[64:67], v[40:43], a[180:183], v[64:67]\n" \
    "v_mfma_f32_16x16x32_bf16 v[68:71], v[36:39], a[188:191], v[68:71]\n" \
    "v_mfma_f32_16x16x32_bf16 v[72:75], v[40:43], a[188:191], v[72:75]\n" \
    "s_cbranch_vccnz LOvg_%=\n" \
    "LOwg_%=:\n" \
    "s_waitcnt lgkmcnt(0)\n" \
    "v_mfma_f32_16x16x32_bf16 v[44:47], v[28:31], a[192:195], v[44:47]\n" \
    "ds_read_b128 v[36:39], v18 offset:1024\n" \
    "ds_read_b128 v[40:43], v18 offset:15360\n" \
    "v_mfma_f32_16x16x32_bf16 v[48:51], v[32:35], a[192:195], v[48:51]\n" \
    "v_add_u32 v17, s20, v17\n" \
    "v_mfma_f32_16x16x32_bf16 v[52:55], v[28:31], a[200:203], v[52:55]\n" \
    "v_mfma_f32_16x16x32_bf16 v[56:59], v[32:35], a[200:203], v[56:59]\n" \
    "v_mfma_f32_16x16x32_bf16 v[60:63], v[28:31], a[208:211], v[60:63]\n" \
    "v_mfma_f32_16x16x32_bf16 v[64:67], v[32:35], a[208:211], v[64:67]\n" \
    "v_mfma_f32_16x16x32_bf16 v[68:71], v[28:31], a[216:219], v[68:71]\n" \
    "v_mfma_f32_16x16x32_bf16 v[72:75], v[32:35], a[216:219], v[72:75]\n" \
    "s_waitcnt lgkmcnt(0)\n" \
    "v_mfma_f32_16x16x32_bf16 v[44:47], v[36:39], a[196:199], v[44:47]\n" \
    "v_add_u32 v18, s20, v18\n" \
    "s_sub_u32 s20, 0, s20\n" \
    "v_mfma_f32_16x16x32_bf16 v[48:51], v[40:43], a[196:199], v[48:51]\n" \
    "v_mfma_f32_16x16x32_bf16 v[52:55], v[36:39], a[204:207], v[52:55]\n" \
    "v_mfma_f32_16x16x32_bf16 v[56:59], v[40:43], a[204:207], v[56:59]\n" \
    "v_mfma_f32_16x16x32_bf16 v[60:63], v[36:39], a[212:215], v[60:63]\n" \
    "v_mfma_f32_16x16x32_bf16 v[64:67], v[40:43], a[212:215], v[64:67]\n" \
    "v_mfma_f32_16x16x32_bf16 v[68:71], v[36:39], a[220:223], v[68:71]\n" \
    "v_mfma_f32_16x16x32_bf16 v[72:75], v[40:43], a[220:223], v[72:75]\n" \
    "v_cvt_pk_bf16_f32 v76, v44, v45\n" \
    "v_cvt_pk_bf16_f32 v78, v46, v47\n" \
    "v_cvt_pk_bf16_f32 v80, v48, v49\n" \
    "v_cvt_pk_bf16_f32 v82, v50, v51\n" \
    "v_cvt_pk_bf16_f32 v77, v52, v53\n" \
    "v_cvt_pk_bf16_f32 v79, v54, v55\n" \
    "v_cvt_pk_bf16_f32 v81, v56, v57\n" \
    "v_cvt_pk_bf16_f32 v83, v58, v59\n" \
    "v_cvt_pk_bf16_f32 v84, v60, v61\n" \
    "v_cvt_pk_bf16_f32 v86, v62, v63\n" \
    "v_cvt_pk_bf16_f32 v88, v64, v65\n" \
    "v_cvt_pk_bf16_f32 v90, v66, v67\n" \
    "v_cvt_pk_bf16_f32 v85, v68, v69\n" \
    "v_cvt_pk_bf16_f32 v87, v70, v71\n" \
    "v_cvt_pk_bf16_f32 v89, v72, v73\n" \
    "v_cvt_pk_bf16_f32 v91, v74, v75\n" \
    "s_cmp_lg_u32 s14, 1\n" \
    "s_cbranch_scc1 LOgo_%=\n" \
    "s_cmp_eq_u32 s13, 0\n" \
    "s_cbranch_scc1 LOgo_%=\n" \
    "s_branch LOpoll1_%=\n" \
    "LOpoll_%=:\n" \
    "s_sub_u32 s17, s17, 1\n" \
    "s_cmp_lt_i32 s17, 0\n" \
    "s_cbranch_scc1 LOpolled_%=\n" \
    "s_sleep 1\n" \
    "buffer_load_dword v153, v8, s[4:7], s10 offen sc1\n" \
    "LOpoll1_%=:\n" \
    "s_waitcnt vmcnt(0)\n" \
    "v_max_u32_dpp v153, v153, v153 quad_perm:[1,0,3,2] row_mask:0xf bank_mask:0xf\n" \
    "s_nop 1\n" \
    "v_max_u32_dpp v153, v153, v153 quad_perm:[2,3,0,1] row_mask:0xf bank_mask:0xf\n" \
    "s_nop 1\n" \
    "v_max_u32_dpp v153, v153, v153 row_half_mirror row_mask:0xf bank_mask:0xf\n" \
    "s_nop 1\n" \
    "v_max_u32_dpp v153, v153, v153 row_mirror row_mask:0xf bank_mask:0xf\n" \
    "s_nop 0\n" \
    "v_readfirstlane_b32 s30, v153\n" \
    "s_nop 0\n" \
    "s_cmp_eq_u32 s30, -1\n" \
    "s_cbranch_scc1 LOpoll_%=\n" \
    "LOpolled_%=:\n" \
    "s_add_u32 s31, s30, s82\n" \
    "s_getpc_b64 s[36:37]\n" \
    "LOfa0_%=:\n" \
    "s_add_u32 s36, s36, LOfa9_%=-LOfa0_%=\n" \
    "s_addc_u32 s37, s37, 0\n" \
    "s_sub_u32 s31, s31, 270\n" \
    "LOfa1_%=:\n" \
    "s_memrealtime s[34:35]\n" \
    "s_sub_u32 s17, s17, 1\n" \
    "s_waitcnt lgkmcnt(0)\n" \
    "s_mul_i32 s34, s34, 24\n" \
    "s_sub_u32 s34, s31, s34\n" \
    "s_min_i32 s34, s34, s17\n" \
    "s_cmpk_gt_i32 s34, 250\n" \
    "s_cbranch_scc1 LOfa1_%=\n" \
    "s_memrealtime s[42:43]\n" \
    "s_nop 0\n" \
    "s_memrealtime s[44:45]\n" \
    "s_nop 0\n" \
    "s_memrealtime s[46:47]\n" \
    "s_nop 0\n" \
    "s_memrealtime s[48:49]\n" \
    "s_nop 0\n" \
    "s_memrealtime s[50:51]\n" \
    "s_nop 0\n" \
    "s_memrealtime s[52:53]\n" \
    "s_nop 0\n" \
    "s_memrealtime s[54:55]\n" \
    "s_nop 0\n" \
    "s_memrealtime s[56:57]\n" \
    "s_nop 0\n" \
    "s_memrealtime s[58:59]\n" \
    "s_nop 0\n" \
    "s_memrealtime s[60:61]\n" \
    "s_nop 0\n" \
    "s_memrealtime s[62:63]\n" \
    "s_nop 0\n" \
    "s_memrealtime s[64:65]\n" \
    "s_nop 0\n" \
    "s_waitcnt lgkmcnt(0)\n" \
    "s_add_u32 s42, s42, s44\n" \
    "s_add_u32 s42, s42, s46\n" \
    "s_add_u32 s42, s42, s48\n" \
    "s_add_u32 s42, s42, s50\n" \
    "s_add_u32 s42, s42, s52\n" \
    "s_add_u32 s42, s42, s54\n" \
    "s_add_u32 s42, s42, s56\n" \
    "s_add_u32 s42, s42, s58\n" \
    "s_add_u32 s42, s42, s60\n" \
    "s_add_u32 s42, s42, s62\n" \
    "s_add_u32 s42, s42, s64\n" \
    "s_lshl_b32 s42, s42, 1\n" \
    "s_sub_u32 s34, s31, s42\n" \
    "s_max_i32 s34, s34, 0\n" \
    "s_min_i32 s34, s34, 252\n" \
    "s_and_b32 s34, s34, 0xfc\n" \
    "s_sub_u32 s36, s36, s34\n" \
    "s_subb_u32 s37, s37, 0\n" \
    "s_setpc_b64 s[36:37]\n" \
    ".p2align 6\n" \
    "s_nop 0\n" \
    "s_nop 0\n" \
    "s_nop 0\n" \
    "s_nop 0\n" \
    "s_nop 0\n" \
    "s_nop 0\n" \
    "s_nop 0\n" \
    "s_nop 0\n" \
    "s_nop 0\n" \
    "s_nop 0\n" \
    "s_nop 0\n" \
    "s_nop 0\n" \
    "s_nop 0\n" \
    "s_nop 0\n" \
    "s_nop 0\n" \
    "s_nop 0\n" \
    "s_nop 0\n" \
    "s_nop 0\n" \
    "s_nop 0\n" \
    "s_nop 0\n" \
    "s_nop 0\n" \
    "s_nop 0\n" \
    "s_nop 0\n" \
    "s_nop 0\n" \
    "s_nop 0\n" \
    "s_nop 0\n" \
    "s_nop 0\n" \
    "s_nop 0\n" \
    "s_nop 0\n" \
    "s_nop 0\n" \
    "s_nop 0\n" \
    "s_nop 0\n" \
    "s_nop 0\n" \
    "s_nop 0\n" \
    "s_nop 0\n" \
    "s_nop 0\n" \
    "s_nop 0\n" \
    "s_nop 0\n" \
    "s_nop 0\n" \
    "s_nop 0\n" \
    "s_nop 0\n" \
    "s_nop 0\n" \
    "s_nop 0\n" \
    "s_nop 0\n" \
    "s_nop 0\n" \
    "s_nop 0\n" \
    "s_nop 0\n" \
    "s_nop 0\n" \
    "s_nop 0\n" \
    "s_nop 0\n" \
    "s_nop 0\n" \
    "s_nop 0\n" \
    "s_nop 0\n" \
    "s_nop 0\n" \
    "s_nop 0\n" \
    "s_nop 0\n" \
    "s_nop 0\n" \
    "s_nop 0\n" \
    "s_nop 0\n" \
    "s_nop 0\n" \
    "s_nop 0\n" \
    "s_nop 0\n" \
    "s_nop 0\n" \
    "s_nop 0\n" \
    "LOfa9_%=:\n" \
    "LOgo_%=:\n" \
    "s_barrier\n" \
    "LOmem_%=:\n" \
    "buffer_load_dwordx4 v[92:95], v10, s[4:7], s21 offen sc1\n" \
    "buffer_load_dwordx4 v[96:99], v11, s[4:7], s21 offen sc1\n" \
    "s_sub_u32 s17, s17, 1\n" \
    "s_cmp_lt_i32 s17, 0\n" \
    "s_cselect_b64 s[28:29], 0, -1\n" \
    "s_waitcnt vmcnt(0)\n" \
    "ds_write_b128 v20, v[92:95]\n" \
    "ds_write_b128 v21, v[96:99]\n" \
    "v_max3_u32 v152, v92, v93, v94\n" \
    "v_max3_u32 v152, v152, v95, v96\n" \
    "v_max3_u32 v152, v152, v97, v98\n" \
    "v_max_u32 v152, v152, v99\n" \
    "v_cmp_eq_u32 vcc, -1, v152\n" \
    "s_nop 0\n" \
    "s_and_b64 vcc, vcc, s[28:29]\n" \
    "s_cbranch_scc1 LOmem_%=\n" \
    "s_add_u32 s26, s22, 4096\n" \
    "s_waitcnt lgkmcnt(0)\n" \
    "s_barrier\n" \
    "ds_read_b128 v[100:103], v22\n" \
    "v_smfmac_f32_16x16x64_bf16 v[120:123], v[92:95], a[0:7], v2\n" \
    "ds_read_b128 v[104:107], v23\n" \
    "v_smfmac_f32_16x16x64_bf16 v[124:127], v[92:95], a[8:15], v2\n" \
    "ds_read_b128 v[108:111], v24\n" \
    "v_smfmac_f32_16x16x64_bf16 v[128:131], v[92:95], a[16:23], v2\n" \
    "ds_read_b128 v[112:115], v25\n" \
    "v_smfmac_f32_16x16x64_bf16 v[132:135], v[92:95], a[24:31], v2\n" \
    "ds_read_b128 v[116:119], v26\n" \
    "v_smfmac_f32_16x16x64_bf16 v[120:123], v[96:99], a[32:39], v2\n" \
    "v_smfmac_f32_16x16x64_bf16 v[124:127], v[96:99], a[40:47], v2\n" \
    "v_smfmac_f32_16x16x64_bf16 v[128:131], v[96:99], a[48:55], v2\n" \
    "v_smfmac_f32_16x16x64_bf16 v[132:135], v[96:99], a[56:63], v2\n" \
    "s_waitcnt lgkmcnt(4)\n" \
    "v_smfmac_f32_16x16x64_bf16 v[120:123], v[100:103], a[64:71], v2\n" \
    "v_smfmac_f32_16x16x64_bf16 v[124:127], v[100:103], a[72:79], v2\n" \
    "v_smfmac_f32_16x16x64_bf16 v[128:131], v[100:103], a[80:87], v2\n" \
    "v_smfmac_f32_16x16x64_bf16 v[132:135], v[100:103], a[88:95], v2\n" \
    "s_waitcnt lgkmcnt(3)\n" \
    "v_smfmac_f32_16x16x64_bf16 v[120:123], v[104:107], a[96:103], v2\n" \
    "v_smfmac_f32_16x16x64_bf16 v[124:127], v[104:107], a[104:111], v2\n" \
    "buffer_load_dword v153, v19, s[4:7], s26 offen sc1\n" \
    "v_smfmac_f32_16x16x64_bf16 v[128:131], v[104:107], a[112:119], v2\n" \
    "v_smfmac_f32_16x16x64_bf16 v[132:135], v[104:107], a[120:127], v2\n" \
    "s_waitcnt lgkmcnt(2)\n" \
    "v_smfmac_f32_16x16x64_bf16 v[120:123], v[108:111], a[128:135], v2\n" \
    "v_smfmac_f32_16x16x64_bf16 v[124:127], v[108:111], a[136:143], v2\n" \
    "v_smfmac_f32_16x16x64_bf16 v[128:131], v[108:111], a[144:151], v2\n" \
    "v_smfmac_f32_16x16x64_bf16 v[132:135], v[108:111], a[152:159], v2\n" \
    "s_waitcnt lgkmcnt(0)\n" \
    "v_smfmac_f32_16x16x64_bf16 v[120:123], v[112:115], a[160:167], v2\n" \
    "s_cmp_lg_u32 s14, 0\n" \
    "s_cbranch_scc0 LOzero_%=\n" \
    "v_smfmac_f32_16x16x64_bf16 v[124:127], v[112:115], a[168:175], v2\n" \
    "v_smfmac_f32_16x16x64_bf16 v[128:131], v[112:115], a[176:183], v2\n" \
    "v_smfmac_f32_16x16x64_bf16 v[132:135], v[112:115], a[184:191], v2\n" \
    "v_smfmac_f32_16x16x64_bf16 v[120:123], v[116:119], a[192:199], v2\n" \
    "v_smfmac_f32_16x16x64_bf16 v[124:127], v[116:119], a[200:207], v2\n" \
    "v_smfmac_f32_16x16x64_bf16 v[128:131], v[116:119], a[208:215], v2\n" \
    "v_smfmac_f32_16x16x64_bf16 v[132:135], v[116:119], a[216:223], v2\n" \
    "buffer_load_dwordx4 v[92:95], v7, s[4:7], s23 offen sc1\n" \
    "buffer_load_dwordx4 v[96:99], v7, s[4:7], s23 offen offset:1024 sc1\n" \
    "s_nop 2\n" \
    "v_add_f32 v120, v120, v121\n" \
    "v_add_f32 v122, v122, v123\n" \
    "v_cvt_pk_bf16_f32 v144, v120, v122\n" \
    "v_add_f32 v124, v124, v125\n" \
    "v_add_f32 v126, v126, v127\n" \
    "v_cvt_pk_bf16_f32 v145, v124, v126\n" \
    "v_add_f32 v128, v128, v129\n" \
    "v_add_f32 v130, v130, v131\n" \
    "v_cvt_pk_bf16_f32 v146, v128, v130\n" \
    "v_add_f32 v132, v132, v133\n" \
    "v_add_f32 v134, v134, v135\n" \
    "v_cvt_pk_bf16_f32 v147, v132, v134\n" \
    "ds_write2st64_b32 v9, v144, v145 offset0:0 offset1:2\n" \
    "ds_write2st64_b32 v9, v146, v147 offset0:4 offset1:6\n" \
    "s_waitcnt vmcnt(0)\n" \
    "v_smfmac_f32_16x16x64_bf16 v[136:139], v[92:95], v[76:83], v6\n" \
    "v_smfmac_f32_16x16x64_bf16 v[140:143], v[96:99], v[84:91], v6\n" \
    "v_max3_u32 v152, v92, v93, v94\n" \
    "v_max3_u32 v152, v152, v95, v96\n" \
    "v_max3_u32 v152, v152, v97, v98\n" \
    "v_max_u32 v152, v152, v99\n" \
    "v_cmp_eq_u32 vcc, -1, v152\n" \
    "s_cbranch_vccnz LOexno_%=\n" \
    "LOexok_%=:\n" \
    "s_nop 0\n" \
    "v_cvt_pk_bf16_f32 v136, v136, v137\n" \
    "v_cvt_pk_bf16_f32 v137, v138, v139\n" \
    "ds_write2_b32 v148, v136, v137 offset0:1 offset1:3\n" \
    "v_cvt_pk_bf16_f32 v140, v140, v141\n" \
    "v_cvt_pk_bf16_f32 v141, v142, v143\n" \
    "ds_write2_b32 v149, v140, v141 offset0:1 offset1:3\n" \
    "s_waitcnt lgkmcnt(0)\n" \
    "s_barrier\n" \
    "s_branch LOnext_%=\n" \
    "LOzero_%=:\n" \
    "v_smfmac_f32_16x16x64_bf16 v[124:127], v[112:115], a[168:175], v2\n" \
    "s_nop 0\n" \
    "s_nop 0\n" \
    "s_memtime s[40:41]\n" \
    "v_smfmac_f32_16x16x64_bf16 v[128:131], v[112:115], a[176:183], v2\n" \
    "s_memrealtime s[42:43]\n" \
    "s_nop 0\n" \
    "s_memrealtime s[44:45]\n" \
    "v_smfmac_f32_16x16x64_bf16 v[132:135], v[112:115], a[184:191], v2\n" \
    "s_memrealtime s[46:47]\n" \
    "s_nop 0\n" \
    "s_memrealtime s[48:49]\n" \
    "v_smfmac_f32_16x16x64_bf16 v[120:123], v[116:119], a[192:199], v2\n" \
    "s_memrealtime s[50:51]\n" \
    "s_nop 0\n" \
    "s_memrealtime s[52:53]\n" \
    "v_smfmac_f32_16x16x64_bf16 v[124:127], v[116:119], a[200:207], v2\n" \
    "s_memrealtime s[54:55]\n" \
    "s_nop 0\n" \
    "s_memrealtime s[56:57]\n" \
    "v_smfmac_f32_16x16x64_bf16 v[128:131], v[116:119], a[208:215], v2\n" \
    "s_memrealtime s[58:59]\n" \
    "s_nop 0\n" \
    "s_memrealtime s[60:61]\n" \
    "v_smfmac_f32_16x16x64_bf16 v[132:135], v[116:119], a[216:223], v2\n" \
    "s_memrealtime s[62:63]\n" \
    "s_nop 0\n" \
    "s_memrealtime s[64:65]\n" \
    "buffer_load_dwordx4 v[92:95], v7, s[4:7], s23 offen sc1\n" \
    "buffer_load_dwordx4 v[96:99], v7, s[4:7], s23 offen offset:1024 sc1\n" \
    "s_waitcnt lgkmcnt(0)\n" \
    "s_add_u32 s42, s42, s44\n" \
    "s_add_u32 s42, s42, s46\n" \
    "s_add_u32 s42, s42, s48\n" \
    "s_add_u32 s42, s42, s50\n" \
    "s_add_u32 s42, s42, s52\n" \
    "s_add_u32 s42, s42, s54\n" \
    "s_add_u32 s42, s42, s56\n" \
    "s_add_u32 s42, s42, s58\n" \
    "s_add_u32 s42, s42, s60\n" \
    "s_add_u32 s42, s42, s62\n" \
    "s_add_u32 s42, s42, s64\n" \
    "s_lshl_b32 s42, s42, 1\n" \
    "s_sub_u32 s18, s42, s40\n" \
    "v_add_f32 v120, v120, v121\n" \
    "v_add_f32 v122, v122, v123\n" \
    "v_cvt_pk_bf16_f32 v144, v120, v122\n" \
    "v_add_f32 v124, v124, v125\n" \
    "v_add_f32 v126, v126, v127\n" \
    "v_cvt_pk_bf16_f32 v145, v124, v126\n" \
    "v_add_f32 v128, v128, v129\n" \
    "v_add_f32 v130, v130, v131\n" \
    "v_cvt_pk_bf16_f32 v146, v128, v130\n" \
    "v_add_f32 v132, v132, v133\n" \
    "v_add_f32 v134, v134, v135\n" \
    "v_cvt_pk_bf16_f32 v147, v132, v134\n" \
    "ds_write2st64_b32 v9, v144, v145 offset0:0 offset1:2\n" \
    "ds_write2st64_b32 v9, v146, v147 offset0:4 offset1:6\n" \
    "s_waitcnt vmcnt(2)\n" \
    "LOeta_%=:\n" \
    "v_max_u32_dpp v153, v153, v153 quad_perm:[1,0,3,2] row_mask:0xf bank_mask:0xf\n" \
    "s_nop 1\n" \
    "v_max_u32_dpp v153, v153, v153 quad_perm:[2,3,0,1] row_mask:0xf bank_mask:0xf\n" \
    "s_nop 1\n" \
    "v_max_u32_dpp v153, v153, v153 row_half_mirror row_mask:0xf bank_mask:0xf\n" \
    "s_nop 1\n" \
    "v_max_u32_dpp v153, v153, v153 row_mirror row_mask:0xf bank_mask:0xf\n" \
    "s_nop 0\n" \
    "v_readfirstlane_b32 s27, v153\n" \
    "s_nop 0\n" \
    "s_cmp_eq_u32 s27, -1\n" \
    "s_cbranch_scc1 LOetano_%=\n" \
    "LOetaok_%=:\n" \
    "s_add_u32 s27, s27, s76\n" \
    "s_sub_u32 s76, s76, 2\n" \
    "s_max_i32 s76, s76, 0\n" \
    "s_sub_u32 s31, s27, s18\n" \
    "s_add_u32 s31, s31, s81\n" \
    "s_sub_u32 s24, s31, s40\n" \
    "s_cmpk_gt_i32 s24, 6000\n" \
    "s_cselect_b32 s31, s40, s31\n" \
    "s_getpc_b64 s[36:37]\n" \
    "LOfc0_%=:\n" \
    "s_add_u32 s36, s36, LOfc9_%=-LOfc0_%=+128\n" \
    "s_addc_u32 s37, s37, 0\n" \
    "LOfc1_%=:\n" \
    "s_memtime s[34:35]\n" \
    "s_waitcnt lgkmcnt(0)\n" \
    "s_sub_u32 s34, s31, s34\n" \
    "s_cmpk_gt_i32 s34, 255\n" \
    "s_cbranch_scc1 LOfc1_%=\n" \
    "s_max_i32 s34, s34, 128\n" \
    "s_and_b32 s34, s34, 0xfc\n" \
    "s_sub_u32 s36, s36, s34\n" \
    "s_subb_u32 s37, s37, 0\n" \
    "s_setpc_b64 s[36:37]\n" \
    ".p2align 6\n" \
    "s_nop 0\n" \
    "s_nop 0\n" \
    "s_nop 0\n" \
    "s_nop 0\n" \
    "s_nop 0\n" \
    "s_nop 0\n" \
    "s_nop 0\n" \
    "s_nop 0\n" \
    "s_nop 0\n" \
    "s_nop 0\n" \
    "s_nop 0\n" \
    "s_nop 0\n" \
    "s_nop 0\n" \
    "s_nop 0\n" \
    "s_nop 0\n" \
    "s_nop 0\n" \
    "s_nop 0\n" \
    "s_nop 0\n" \
    "s_nop 0\n" \
    "s_nop 0\n" \
    "s_nop 0\n" \
    "s_nop 0\n" \
    "s_nop 0\n" \
    "s_nop 0\n" \
    "s_nop 0\n" \
    "s_nop 0\n" \
    "s_nop 0\n" \
    "s_nop 0\n" \
    "s_nop 0\n" \
    "s_nop 0\n" \
    "s_nop 0\n" \
    "s_nop 0\n" \
    "LOfc9_%=:\n" \
    "buffer_load_dwordx4 v[192:195], v1, s[4:7], s22 offen offset:1024 sc1\n" \
    "buffer_load_dwordx4 v[196:199], v1, s[4:7], s22 offen offset:2048 sc1\n" \
    "buffer_load_dwordx4 v[200:203], v1, s[4:7], s22 offen offset:3072 sc1\n" \
    "buffer_load_dwordx4 v[204:207], v1, s[4:7], s22 offen offset:0 sc1\n" \
    "s_waitcnt vmcnt(4)\n" \
    "v_smfmac_f32_16x16x64_bf16 v[136:139], v[92:95], v[76:83], v6\n" \
    "v_smfmac_f32_16x16x64_bf16 v[140:143], v[96:99], v[84:91], v6\n" \
    "v_max3_u32 v152, v92, v93, v94\n" \
    "v_max3_u32 v152, v152, v95, v96\n" \
    "v_max3_u32 v152, v152, v97, v98\n" \
    "v_max_u32 v152, v152, v99\n" \
    "v_cmp_eq_u32 vcc, -1, v152\n" \
    "s_cbranch_vccnz LOexno_%=\n" \
    "LOexzok_%=:\n" \
    "s_nop 0\n" \
    "v_cvt_pk_bf16_f32 v136, v136, v137\n" \
    "v_cvt_pk_bf16_f32 v137, v138, v139\n" \
    "ds_write2_b32 v148, v136, v137 offset0:1 offset1:3\n" \
    "v_cvt_pk_bf16_f32 v140, v140, v141\n" \
    "v_cvt_pk_bf16_f32 v141, v142, v143\n" \
    "ds_write2_b32 v149, v140, v141 offset0:1 offset1:3\n" \
    "s_barrier\n" \
    "ds_read_b128 v[160:163], v3 offset:2048\n" \
    "ds_read_b128 v[164:167], v3 offset:3072\n" \
    "ds_read_b128 v[168:171], v3 offset:4096\n" \
    "ds_read_b128 v[172:175], v3 offset:5120\n" \
    "ds_read_b128 v[176:179], v3 offset:6144\n" \
    "ds_read_b128 v[180:183], v3 offset:7168\n" \
    "ds_read_b128 v[184:187], v3 offset:0\n" \
    "ds_read_b128 v[188:191], v3 offset:1024\n" \
    "LOpv_%=:\n" \
    "v_mov_b32 v156, 0\n" \
    "v_mov_b32 v157, 0\n" \
    "v_mov_b32 v158, 0\n" \
    "v_mov_b32 v159, 0\n" \
    "s_waitcnt vmcnt(3) lgkmcnt(6)\n" \
    "s_nop 0\n" \
    "v_smfmac_f32_16x16x64_bf16 v[156:159], v[192:195], v[160:167], v2\n" \
    "s_waitcnt vmcnt(2) lgkmcnt(4)\n" \
    "v_max3_u32 v152, v192, v193, v194\n" \
    "v_smfmac_f32_16x16x64_bf16 v[156:159], v[196:199], v[168:175], v2\n" \
    "s_waitcnt vmcnt(1) lgkmcnt(2)\n" \
    "v_max3_u32 v152, v152, v195, v196\n" \
    "v_smfmac_f32_16x16x64_bf16 v[156:159], v[200:203], v[176:183], v2\n" \
    "v_max3_u32 v152, v152, v197, v198\n" \
    "s_waitcnt vmcnt(0) lgkmcnt(0)\n" \
    "v_smfmac_f32_16x16x64_bf16 v[156:159], v[204:207], v[184:191], v2\n" \
    "s_nop 1\n" \
    "v_max3_u32 v152, v152, v199, v200\n" \
    "v_max3_u32 v152, v152, v201, v202\n" \
    "v_max3_u32 v152, v152, v203, v204\n" \
    "v_max3_u32 v152, v152, v205, v206\n" \
    "v_max_u32 v152, v152, v207\n" \
    "v_cmp_eq_u32 vcc, -1, v152\n" \
    "v_add_f32 v156, v156, v157\n" \
    "v_add_f32 v158, v158, v159\n" \
    "v_cvt_pk_bf16_f32 v156, v156, v158\n" \
    "s_cbranch_vccnz LOagain_%=\n" \
    "LOpvok_%=:\n" \
    "s_nop 0\n" \
    "v_mov_b32_dpp v157, v156 quad_perm:[1,0,3,2] row_mask:0xf bank_mask:0xf\n" \
    "v_perm_b32 v156, v157, v156, v5\n" \
    "v_min_u32 v156, -2, v156\n" \
    "buffer_store_dword v156, v4, s[4:7], s38 offen sc1\n" \
    "buffer_store_dword v156, v4, s[4:7], s39 offen sc1\n" \
    "LOnext_%=:\n" \
    "s_barrier\n" \
    "s_add_u32 s13, s13, 2\n" \
    "s_cmp_lt_u32 s13, 63\n" \
    "s_cbranch_scc1 LOturn_%=\n" \
    "s_endpgm\n" \
    "LOva_%=:\n" \
    "s_lshl_b32 s2, s1, 11\n" \
    "s_add_u32 s2, s2, s0\n" \
    "s_add_u32 s1, s1, 1\n" \
    "s_cmp_eq_u32 s1, 7\n" \
    "s_cselect_b32 s1, 0, s1\n" \
    "buffer_load_dwordx4 a[64:67], v1, s[4:7], s2 offen offset:0\n" \
    "buffer_load_dwordx4 a[68:71], v1, s[4:7], s2 offen offset:1024\n" \
    "s_add_u32 s2, s2, 401408\n" \
    "buffer_load_dwordx4 a[72:75], v1, s[4:7], s2 offen offset:0\n" \
    "buffer_load_dwordx4 a[76:79], v1, s[4:7], s2 offen offset:1024\n" \
    "s_add_u32 s2, s2, 401408\n" \
    "buffer_load_dwordx4 a[80:83], v1, s[4:7], s2 offen offset:0\n" \
    "buffer_load_dwordx4 a[84:87], v1, s[4:7], s2 offen offset:1024\n" \
    "s_add_u32 s2, s2, 401408\n" \
    "buffer_load_dwordx4 a[88:91], v1, s[4:7], s2 offen offset:0\n" \
    "buffer_load_dwordx4 a[92:95], v1, s[4:7], s2 offen offset:1024\n" \
    "s_waitcnt vmcnt(16)\n" \
    "s_branch LOwa_%=\n" \
    "LOvb_%=:\n" \
    "s_lshl_b32 s2, s1, 11\n" \
    "s_add_u32 s2, s2, s0\n" \
    "s_add_u32 s1, s1, 1\n" \
    "s_cmp_eq_u32 s1, 7\n" \
    "s_cselect_b32 s1, 0, s1\n" \
    "buffer_load_dwordx4 a[96:99], v1, s[4:7], s2 offen offset:0\n" \
    "buffer_load_dwordx4 a[100:103], v1, s[4:7], s2 offen offset:1024\n" \
    "s_add_u32 s2, s2, 401408\n" \
    "buffer_load_dwordx4 a[104:107], v1, s[4:7], s2 offen offset:0\n" \
    "buffer_load_dwordx4 a[108:111], v1, s[4:7], s2 offen offset:1024\n" \
    "s_add_u32 s2, s2, 401408\n" \
    "buffer_load_dwordx4 a[112:115], v1, s[4:7], s2 offen offset:0\n" \
    "buffer_load_dwordx4 a[116:119], v1, s[4:7], s2 offen offset:1024\n" \
    "s_add_u32 s2, s2, 401408\n" \
    "buffer_load_dwordx4 a[120:123], v1, s[4:7], s2 offen offset:0\n" \
    "buffer_load_dwordx4 a[124:127], v1, s[4:7], s2 offen offset:1024\n" \
    "s_waitcnt vmcnt(16)\n" \
    "s_branch LOwb_%=\n" \
    "LOvc_%=:\n" \
    "s_lshl_b32 s2, s1, 11\n" \
    "s_add_u32 s2, s2, s0\n" \
    "s_add_u32 s1, s1, 1\n" \
    "s_cmp_eq_u32 s1, 7\n" \
    "s_cselect_b32 s1, 0, s1\n" \
    "buffer_load_dwordx4 a[128:131], v1, s[4:7], s2 offen offset:0\n" \
    "buffer_load_dwordx4 a[132:135], v1, s[4:7], s2 offen offset:1024\n" \
    "s_add_u32 s2, s2, 401408\n" \
    "buffer_load_dwordx4 a[136:139], v1, s[4:7], s2 offen offset:0\n" \
    "buffer_load_dwordx4 a[140:143], v1, s[4:7], s2 offen offset:1024\n" \
    "s_add_u32 s2, s2, 401408\n" \
    "buffer_load_dwordx4 a[144:147], v1, s[4:7], s2 offen offset:0\n" \
    "buffer_load_dwordx4 a[148:151], v1, s[4:7], s2 offen offset:1024\n" \
    "s_add_u32 s2, s2, 401408\n" \
    "buffer_load_dwordx4 a[152:155], v1, s[4:7], s2 offen offset:0\n" \
    "buffer_load_dwordx4 a[156:159], v1, s[4:7], s2 offen offset:1024\n" \
    "s_waitcnt vmcnt(16)\n" \
    "s_branch LOwc_%=\n" \
    "LOvd_%=:\n" \
    "s_lshl_b32 s2, s1, 11\n" \
    "s_add_u32 s2, s2, s0\n" \
    "s_add_u32 s1, s1, 1\n" \
    "s_cmp_eq_u32 s1, 7\n" \
    "s_cselect_b32 s1, 0, s1\n" \
    "buffer_load_dwordx4 a[160:163], v1, s[4:7], s2 offen offset:0\n" \
    "buffer_load_dwordx4 a[164:167], v1, s[4:7], s2 offen offset:1024\n" \
    "s_add_u32 s2, s2, 401408\n" \
    "buffer_load_dwordx4 a[168:171], v1, s[4:7], s2 offen offset:0\n" \
    "buffer_load_dwordx4 a[172:175], v1, s[4:7], s2 offen offset:1024\n" \
    "s_add_u32 s2, s2, 401408\n" \
    "buffer_load_dwordx4 a[176:179], v1, s[4:7], s2 offen offset:0\n" \
    "buffer_load_dwordx4 a[180:183], v1, s[4:7], s2 offen offset:1024\n" \
    "s_add_u32 s2, s2, 401408\n" \
    "buffer_load_dwordx4 a[184:187], v1, s[4:7], s2 offen offset:0\n" \
    "buffer_load_dwordx4 a[188:191], v1, s[4:7], s2 offen offset:1024\n" \
    "s_waitcnt vmcnt(16)\n" \
    "s_branch LOwd_%=\n" \
    "LOve_%=:\n" \
    "s_lshl_b32 s2, s1, 11\n" \
    "s_add_u32 s2, s2, s0\n" \
    "s_add_u32 s1, s1, 1\n" \
    "s_cmp_eq_u32 s1, 7\n" \
    "s_cselect_b32 s1, 0, s1\n" \
    "buffer_load_dwordx4 a[192:195], v1, s[4:7], s2 offen offset:0\n" \
    "buffer_load_dwordx4 a[196:199], v1, s[4:7], s2 offen offset:1024\n" \
    "s_add_u32 s2, s2, 401408\n" \
    "buffer_load_dwordx4 a[200:203], v1, s[4:7], s2 offen offset:0\n" \
    "buffer_load_dwordx4 a[204:207], v1, s[4:7], s2 offen offset:1024\n" \
    "s_add_u32 s2, s2, 401408\n" \
    "buffer_load_dwordx4 a[208:211], v1, s[4:7], s2 offen offset:0\n" \
    "buffer_load_dwordx4 a[212:215], v1, s[4:7], s2 offen offset:1024\n" \
    "s_add_u32 s2, s2, 401408\n" \
    "buffer_load_dwordx4 a[216:219], v1, s[4:7], s2 offen offset:0\n" \
    "buffer_load_dwordx4 a[220:223], v1, s[4:7], s2 offen offset:1024\n" \
    "s_waitcnt vmcnt(16)\n" \
    "s_branch LOwe_%=\n" \
    "LOvf_%=:\n" \
    "s_waitcnt vmcnt(8)\n" \
    "s_branch LOwf_%=\n" \
    "LOvg_%=:\n" \
    "s_waitcnt vmcnt(0)\n" \
    "s_branch LOwg_%=\n" \
    "LOagain_%=:\n" \
    "s_add_u32 s76, s76, 24\n" \
    "s_min_i32 s76, s76, 96\n" \
    "s_sub_u32 s17, s17, 1\n" \
    "s_cmp_lt_i32 s17, 0\n" \
    "s_cbranch_scc1 LOpvok_%=\n" \
    "buffer_load_dwordx4 v[192:195], v1, s[4:7], s22 offen offset:1024 sc1\n" \
    "buffer_load_dwordx4 v[196:199], v1, s[4:7], s22 offen offset:2048 sc1\n" \
    "buffer_load_dwordx4 v[200:203], v1, s[4:7], s22 offen offset:3072 sc1\n" \
    "buffer_load_dwordx4 v[204:207], v1, s[4:7], s22 offen offset:0 sc1\n" \
    "s_branch LOpv_%=\n" \
    "LOetano_%=:\n" \
    "s_sub_u32 s17, s17, 1\n" \
    "s_cmp_lt_i32 s17, 0\n" \
    "s_cbranch_scc1 LOetaok_%=\n" \
    "buffer_load_dword v153, v8, s[4:7], s26 offen sc1\n" \
    "s_waitcnt vmcnt(0)\n" \
    "v_max_u32_dpp v153, v153, v153 quad_perm:[1,0,3,2] row_mask:0xf bank_mask:0xf\n" \
    "s_nop 1\n" \
    "v_max_u32_dpp v153, v153, v153 quad_perm:[2,3,0,1] row_mask:0xf bank_mask:0xf\n" \
    "s_nop 1\n" \
    "v_max_u32_dpp v153, v153, v153 row_half_mirror row_mask:0xf bank_mask:0xf\n" \
    "s_nop 1\n" \
    "v_max_u32_dpp v153, v153, v153 row_mirror row_mask:0xf bank_mask:0xf\n" \
    "s_nop 0\n" \
    "v_readfirstlane_b32 s27, v153\n" \
    "s_nop 0\n" \
    "s_cmp_eq_u32 s27, -1\n" \
    "s_cbranch_scc1 LOetano_%=\n" \
    "buffer_load_dwordx4 v[92:95], v7, s[4:7], s23 offen sc1\n" \
    "buffer_load_dwordx4 v[96:99], v7, s[4:7], s23 offen offset:1024 sc1\n" \
    "LOand_%=:\n" \
    "s_mov_b32 s36, 1\n" \
    "LOand1_%=:\n" \
    "s_memtime s[40:41]\n" \
    "s_nop 0\n" \
    "s_memrealtime s[42:43]\n" \
    "s_nop 0\n" \
    "s_memrealtime s[44:45]\n" \
    "s_nop 0\n" \
    "s_memrealtime s[46:47]\n" \
    "s_nop 0\n" \
    "s_memrealtime s[48:49]\n" \
    "s_nop 0\n" \
    "s_memrealtime s[50:51]\n" \
    "s_nop 0\n" \
    "s_memrealtime s[52:53]\n" \
    "s_nop 0\n" \
    "s_memrealtime s[54:55]\n" \
    "s_nop 0\n" \
    "s_memrealtime s[56:57]\n" \
    "s_nop 0\n" \
    "s_memrealtime s[58:59]\n" \
    "s_nop 0\n" \
    "s_memrealtime s[60:61]\n" \
    "s_nop 0\n" \
    "s_memrealtime s[62:63]\n" \
    "s_nop 0\n" \
    "s_memrealtime s[64:65]\n" \
    "s_nop 0\n" \
    "s_memtime s[34:35]\n" \
    "s_sub_u32 s36, s36, 1\n" \
    "s_cselect_b32 s37, -1, 116\n" \
    "s_waitcnt lgkmcnt(0)\n" \
    "s_sub_u32 s34, s34, s40\n" \
    "s_cmp_gt_u32 s34, s37\n" \
    "s_cbranch_scc1 LOand1_%=\n" \
    "s_add_u32 s42, s42, s44\n" \
    "s_add_u32 s42, s42, s46\n" \
    "s_add_u32 s42, s42, s48\n" \
    "s_add_u32 s42, s42, s50\n" \
    "s_add_u32 s42, s42, s52\n" \
    "s_add_u32 s42, s42, s54\n" \
    "s_add_u32 s42, s42, s56\n" \
    "s_add_u32 s42, s42, s58\n" \
    "s_add_u32 s42, s42, s60\n" \
    "s_add_u32 s42, s42, s62\n" \
    "s_add_u32 s42, s42, s64\n" \
    "s_lshl_b32 s42, s42, 1\n" \
    "s_sub_u32 s18, s42, s40\n" \
    "s_branch LOetaok_%=\n" \
    "LOexno_%=:\n" \
    "buffer_load_dwordx4 v[92:95], v7, s[4:7], s23 offen sc1\n" \
    "buffer_load_dwordx4 v[96:99], v7, s[4:7], s23 offen offset:1024 sc1\n" \
    "s_sub_u32 s17, s17, 1\n" \
    "s_cmp_lt_i32 s17, 0\n" \
    "s_cselect_b64 s[28:29], 0, -1\n" \
    "s_waitcnt vmcnt(0)\n" \
    "v_max3_u32 v152, v92, v93, v94\n" \
    "v_max3_u32 v152, v152, v95, v96\n" \
    "v_max3_u32 v152, v152, v97, v98\n" \
    "v_max_u32 v152, v152, v99\n" \
    "v_cmp_eq_u32 vcc, -1, v152\n" \
    "s_nop 0\n" \
    "s_and_b64 vcc, vcc, s[28:29]\n" \
    "s_cbranch_scc1 LOexno_%=\n" \
    "v_mov_b32 v136, 0\n" \
    "v_mov_b32 v137, 0\n" \
    "v_mov_b32 v138, 0\n" \
    "v_mov_b32 v139, 0\n" \
    "v_mov_b32 v140, 0\n" \
    "v_mov_b32 v141, 0\n" \
    "v_mov_b32 v142, 0\n" \
    "v_mov_b32 v143, 0\n" \
    "v_smfmac_f32_16x16x64_bf16 v[136:139], v[92:95], v[76:83], v6\n" \
    "s_nop 0\n" \
    "v_smfmac_f32_16x16x64_bf16 v[140:143], v[96:99], v[84:91], v6\n" \
    "s_nop 7\n" \
    "s_cmp_lg_u32 s14, 0\n" \
    "s_cbranch_scc1 LOexok_%=\n" \
    "LOend_%=:\n" \
    "s_branch LOexzok_%=\n"
#define F_ASM \
    "s_mov_b64 s[4:5], %0\n" \
    "s_mov_b32 s6, %7\n" \
    "s_mov_b32 s7, 0x20000\n" \
    "s_mov_b64 s[8:9], %1\n" \
    "s_mov_b32 s10, 0x1c0000\n" \
    "s_mov_b32 s11, 0x20000\n" \
    "s_mov_b32 s12, %2\n" \
    "s_mov_b32 s13, %3\n" \
    "s_mov_b32 s14, %4\n" \
    "s_mov_b32 s15, %5\n" \
    "s_mov_b32 s16, %6\n" \
    "s_and_b32 s5, s5, 0xffff\n" \
    "s_and_b32 s9, s9, 0xffff\n" \
    "s_mov_b32 s17, 300000\n" \
    "s_mov_b32 s19, 0\n" \
    "s_mov_b32 s76, 0\n" \
    "s_mov_b32 s20, 28672\n" \
    "v_mbcnt_lo_u32_b32 v0, -1, 0\n" \
    "v_mbcnt_hi_u32_b32 v0, -1, v0\n" \
    "v_lshlrev_b32 v1, 4, v0\n" \
    "v_and_b32 v152, 15, v0\n" \
    "s_lshl_b32 s24, s14, 12\n" \
    "v_add_u32 v154, s24, v1\n" \
    "ds_read_b128 v[200:203], v154 offset:0\n" \
    "ds_read_b128 v[204:207], v154 offset:1024\n" \
    "ds_read_b128 v[208:211], v154 offset:2048\n" \
    "ds_read_b128 v[212:215], v154 offset:3072\n" \
    "s_waitcnt lgkmcnt(0)\n" \
    "s_barrier\n" \
    "s_add_u32 s26, s13, 0\n" \
    "s_min_u32 s26, s26, 63\n" \
    "s_lshl_b32 s26, s26, 2\n" \
    "s_add_u32 s26, s26, s14\n" \
    "s_lshl_b32 s26, s26, 14\n" \
    "s_lshr_b32 s27, s26, 8\n" \
    "s_and_b32 s27, s27, 0x2000\n" \
    "s_add_u32 s26, s26, s27\n" \
    "s_add_u32 s26, s26, 44040192\n" \
    "s_mul_i32 s27, s14, 7168\n" \
    "s_add_u32 m0, s27, s19\n" \
    "s_nop 0\n" \
    "buffer_load_dwordx4 v1, s[4:7], s26 offen offset:0 lds\n" \
    "buffer_load_dwordx4 v1, s[4:7], s26 offen offset:1024 lds\n" \
    "buffer_load_dwordx4 v1, s[4:7], s26 offen offset:2048 lds\n" \
    "buffer_load_dwordx4 v1, s[4:7], s26 offen offset:3072 lds\n" \
    "s_add_u32 m0, m0, 4096\n" \
    "s_add_u32 s26, s26, 4096\n" \
    "buffer_load_dwordx4 v1, s[4:7], s26 offen offset:0 lds\n" \
    "buffer_load_dwordx4 v1, s[4:7], s26 offen offset:1024 lds\n" \
    "buffer_load_dwordx4 v1, s[4:7], s26 offen offset:2048 lds\n" \
    "s_lshl_b32 s0, s14, 2\n" \
    "s_add_u32 s0, s0, 16\n" \
    "s_mul_i32 s0, s0, 28\n" \
    "s_add_u32 s0, s0, s12\n" \
    "s_mul_i32 s0, s0, 14336\n" \
    "s_add_u32 s0, s0, 4194304\n" \
    "s_lshl_b32 s1, s14, 1\n" \
    "s_lshl_b32 s2, s1, 11\n" \
    "s_add_u32 s2, s2, s0\n" \
    "s_add_u32 s1, s1, 1\n" \
    "s_cmp_eq_u32 s1, 7\n" \
    "s_cselect_b32 s1, 0, s1\n" \
    "buffer_load_dwordx4 a[0:3], v1, s[4:7], s2 offen offset:0\n" \
    "buffer_load_dwordx4 a[4:7], v1, s[4:7], s2 offen offset:1024\n" \
    "s_add_u32 s2, s2, 401408\n" \
    "buffer_load_dwordx4 a[8:11], v1, s[4:7], s2 offen offset:0\n" \
    "buffer_load_dwordx4 a[12:15], v1, s[4:7], s2 offen offset:1024\n" \
    "s_add_u32 s2, s2, 401408\n" \
    "s_waitcnt vmcnt(0)\n" \
    "s_sleep 8\n" \
    "buffer_load_dwordx4 a[16:19], v1, s[4:7], s2 offen offset:0\n" \
    "buffer_load_dwordx4 a[20:23], v1, s[4:7], s2 offen offset:1024\n" \
    "s_add_u32 s2, s2, 401408\n" \
    "buffer_load_dwordx4 a[24:27], v1, s[4:7], s2 offen offset:0\n" \
    "buffer_load_dwordx4 a[28:31], v1, s[4:7], s2 offen offset:1024\n" \
    "s_waitcnt vmcnt(0)\n" \
    "s_sleep 8\n" \
    "s_lshl_b32 s2, s1, 11\n" \
    "s_add_u32 s2, s2, s0\n" \
    "s_add_u32 s1, s1, 1\n" \
    "s_cmp_eq_u32 s1, 7\n" \
    "s_cselect_b32 s1, 0, s1\n" \
    "buffer_load_dwordx4 a[32:35], v1, s[4:7], s2 offen offset:0\n" \
    "buffer_load_dwordx4 a[36:39], v1, s[4:7], s2 offen offset:1024\n" \
    "s_add_u32 s2, s2, 401408\n" \
    "buffer_load_dwordx4 a[40:43], v1, s[4:7], s2 offen offset:0\n" \
    "buffer_load_dwordx4 a[44:47], v1, s[4:7], s2 offen offset:1024\n" \
    "s_add_u32 s2, s2, 401408\n" \
    "s_waitcnt vmcnt(0)\n" \
    "s_sleep 8\n" \
    "buffer_load_dwordx4 a[48:51], v1, s[4:7], s2 offen offset:0\n" \
    "buffer_load_dwordx4 a[52:55], v1, s[4:7], s2 offen offset:1024\n" \
    "s_add_u32 s2, s2, 401408\n" \
    "buffer_load_dwordx4 a[56:59], v1, s[4:7], s2 offen offset:0\n" \
    "buffer_load_dwordx4 a[60:63], v1, s[4:7], s2 offen offset:1024\n" \
    "s_waitcnt vmcnt(0)\n" \
    "s_sleep 8\n" \
    "s_lshl_b32 s2, s1, 11\n" \
    "s_add_u32 s2, s2, s0\n" \
    "s_add_u32 s1, s1, 1\n" \
    "s_cmp_eq_u32 s1, 7\n" \
    "s_cselect_b32 s1, 0, s1\n" \
    "buffer_load_dwordx4 a[64:67], v1, s[4:7], s2 offen offset:0\n" \
    "buffer_load_dwordx4 a[68:71], v1, s[4:7], s2 offen offset:1024\n" \
    "s_add_u32 s2, s2, 401408\n" \
    "buffer_load_dwordx4 a[72:75], v1, s[4:7], s2 offen offset:0\n" \
    "buffer_load_dwordx4 a[76:79], v1, s[4:7], s2 offen offset:1024\n" \
    "s_add_u32 s2, s2, 401408\n" \
    "s_waitcnt vmcnt(0)\n" \
    "s_sleep 8\n" \
    "buffer_load_dwordx4 a[80:83], v1, s[4:7], s2 offen offset:0\n" \
    "buffer_load_dwordx4 a[84:87], v1, s[4:7], s2 offen offset:1024\n" \
    "s_add_u32 s2, s2, 401408\n" \
    "buffer_load_dwordx4 a[88:91], v1, s[4:7], s2 offen offset:0\n" \
    "buffer_load_dwordx4 a[92:95], v1, s[4:7], s2 offen offset:1024\n" \
    "s_waitcnt vmcnt(0)\n" \
    "s_sleep 8\n" \
    "s_lshl_b32 s2, s1, 11\n" \
    "s_add_u32 s2, s2, s0\n" \
    "s_add_u32 s1, s1, 1\n" \
    "s_cmp_eq_u32 s1, 7\n" \
    "s_cselect_b32 s1, 0, s1\n" \
    "buffer_load_dwordx4 a[96:99], v1, s[4:7], s2 offen offset:0\n" \
    "buffer_load_dwordx4 a[100:103], v1, s[4:7], s2 offen offset:1024\n" \
    "s_add_u32 s2, s2, 401408\n" \
    "buffer_load_dwordx4 a[104:107], v1, s[4:7], s2 offen offset:0\n" \
    "buffer_load_dwordx4 a[108:111], v1, s[4:7], s2 offen offset:1024\n" \
    "s_add_u32 s2, s2, 401408\n" \
    "s_waitcnt vmcnt(0)\n" \
    "s_sleep 8\n" \
    "buffer_load_dwordx4 a[112:115], v1, s[4:7], s2 offen offset:0\n" \
    "buffer_load_dwordx4 a[116:119], v1, s[4:7], s2 offen offset:1024\n" \
    "s_add_u32 s2, s2, 401408\n" \
    "buffer_load_dwordx4 a[120:123], v1, s[4:7], s2 offen offset:0\n" \
    "buffer_load_dwordx4 a[124:127], v1, s[4:7], s2 offen offset:1024\n" \
    "s_waitcnt vmcnt(0)\n" \
    "s_sleep 8\n" \
    "s_lshl_b32 s2, s1, 11\n" \
    "s_add_u32 s2, s2, s0\n" \
    "s_add_u32 s1, s1, 1\n" \
    "s_cmp_eq_u32 s1, 7\n" \
    "s_cselect_b32 s1, 0, s1\n" \
    "buffer_load_dwordx4 a[128:131], v1, s[4:7], s2 offen offset:0\n" \
    "buffer_load_dwordx4 a[132:135], v1, s[4:7], s2 offen offset:1024\n" \
    "s_add_u32 s2, s2, 401408\n" \
    "buffer_load_dwordx4 a[136:139], v1, s[4:7], s2 offen offset:0\n" \
    "buffer_load_dwordx4 a[140:143], v1, s[4:7], s2 offen offset:1024\n" \
    "s_add_u32 s2, s2, 401408\n" \
    "s_waitcnt vmcnt(0)\n" \
    "s_sleep 8\n" \
    "buffer_load_dwordx4 a[144:147], v1, s[4:7], s2 offen offset:0\n" \
    "buffer_load_dwordx4 a[148:151], v1, s[4:7], s2 offen offset:1024\n" \
    "s_add_u32 s2, s2, 401408\n" \
    "buffer_load_dwordx4 a[152:155], v1, s[4:7], s2 offen offset:0\n" \
    "buffer_load_dwordx4 a[156:159], v1, s[4:7], s2 offen offset:1024\n" \
    "s_waitcnt vmcnt(0)\n" \
    "s_sleep 8\n" \
    "s_lshl_b32 s2, s1, 11\n" \
    "s_add_u32 s2, s2, s0\n" \
    "s_add_u32 s1, s1, 1\n" \
    "s_cmp_eq_u32 s1, 7\n" \
    "s_cselect_b32 s1, 0, s1\n" \
    "buffer_load_dwordx4 a[160:163], v1, s[4:7], s2 offen offset:0\n" \
    "buffer_load_dwordx4 a[164:167], v1, s[4:7], s2 offen offset:1024\n" \
    "s_add_u32 s2, s2, 401408\n" \
    "buffer_load_dwordx4 a[168:171], v1, s[4:7], s2 offen offset:0\n" \
    "buffer_load_dwordx4 a[172:175], v1, s[4:7], s2 offen offset:1024\n" \
    "s_add_u32 s2, s2, 401408\n" \
    "s_waitcnt vmcnt(0)\n" \
    "s_sleep 8\n" \
    "buffer_load_dwordx4 a[176:179], v1, s[4:7], s2 offen offset:0\n" \
    "buffer_load_dwordx4 a[180:183], v1, s[4:7], s2 offen offset:1024\n" \
    "s_add_u32 s2, s2, 401408\n" \
    "buffer_load_dwordx4 a[184:187], v1, s[4:7], s2 offen offset:0\n" \
    "buffer_load_dwordx4 a[188:191], v1, s[4:7], s2 offen offset:1024\n" \
    "s_waitcnt vmcnt(0)\n" \
    "s_sleep 8\n" \
    "s_lshl_b32 s2, s1, 11\n" \
    "s_add_u32 s2, s2, s0\n" \
    "s_add_u32 s1, s1, 1\n" \
    "s_cmp_eq_u32 s1, 7\n" \
    "s_cselect_b32 s1, 0, s1\n" \
    "buffer_load_dwordx4 a[192:195], v1, s[4:7], s2 offen offset:0\n" \
    "buffer_load_dwordx4 a[196:199], v1, s[4:7], s2 offen offset:1024\n" \
    "s_add_u32 s2, s2, 401408\n" \
    "buffer_load_dwordx4 a[200:203], v1, s[4:7], s2 offen offset:0\n" \
    "buffer_load_dwordx4 a[204:207], v1, s[4:7], s2 offen offset:1024\n" \
    "s_add_u32 s2, s2, 401408\n" \
    "s_waitcnt vmcnt(0)\n" \
    "s_sleep 8\n" \
    "buffer_load_dwordx4 a[208:211], v1, s[4:7], s2 offen offset:0\n" \
    "buffer_load_dwordx4 a[212:215], v1, s[4:7], s2 offen offset:1024\n" \
    "s_add_u32 s2, s2, 401408\n" \
    "buffer_load_dwordx4 a[216:219], v1, s[4:7], s2 offen offset:0\n" \
    "buffer_load_dwordx4 a[220:223], v1, s[4:7], s2 offen offset:1024\n" \
    "s_waitcnt vmcnt(0)\n" \
    "s_sleep 8\n" \
    "s_lshl_b32 s25, s14, 1\n" \
    "s_lshl_b32 s26, s25, 11\n" \
    "v_add_u32 v12, s26, v1\n" \
    "s_lshl_b32 s27, s25, 10\n" \
    "s_add_u32 s27, s27, 57344\n" \
    "v_add_u32 v20, s27, v1\n" \
    "s_add_u32 s25, s25, 1\n" \
    "s_cmp_eq_u32 s25, 7\n" \
    "s_cselect_b32 s25, 0, s25\n" \
    "s_lshl_b32 s26, s25, 11\n" \
    "v_add_u32 v13, s26, v1\n" \
    "s_lshl_b32 s27, s25, 10\n" \
    "s_add_u32 s27, s27, 57344\n" \
    "v_add_u32 v21, s27, v1\n" \
    "s_add_u32 s25, s25, 1\n" \
    "s_cmp_eq_u32 s25, 7\n" \
    "s_cselect_b32 s25, 0, s25\n" \
    "s_lshl_b32 s26, s25, 11\n" \
    "v_add_u32 v14, s26, v1\n" \
    "s_lshl_b32 s27, s25, 10\n" \
    "s_add_u32 s27, s27, 57344\n" \
    "v_add_u32 v22, s27, v1\n" \
    "s_add_u32 s25, s25, 1\n" \
    "s_cmp_eq_u32 s25, 7\n" \
    "s_cselect_b32 s25, 0, s25\n" \
    "s_lshl_b32 s26, s25, 11\n" \
    "v_add_u32 v15, s26, v1\n" \
    "s_lshl_b32 s27, s25, 10\n" \
    "s_add_u32 s27, s27, 57344\n" \
    "v_add_u32 v23, s27, v1\n" \
    "s_add_u32 s25, s25, 1\n" \
    "s_cmp_eq_u32 s25, 7\n" \
    "s_cselect_b32 s25, 0, s25\n" \
    "s_lshl_b32 s26, s25, 11\n" \
    "v_add_u32 v16, s26, v1\n" \
    "s_lshl_b32 s27, s25, 10\n" \
    "s_add_u32 s27, s27, 57344\n" \
    "v_add_u32 v24, s27, v1\n" \
    "s_add_u32 s25, s25, 1\n" \
    "s_cmp_eq_u32 s25, 7\n" \
    "s_cselect_b32 s25, 0, s25\n" \
    "s_lshl_b32 s26, s25, 11\n" \
    "v_add_u32 v17, s26, v1\n" \
    "s_lshl_b32 s27, s25, 10\n" \
    "s_add_u32 s27, s27, 57344\n" \
    "v_add_u32 v25, s27, v1\n" \
    "s_add_u32 s25, s25, 1\n" \
    "s_cmp_eq_u32 s25, 7\n" \
    "s_cselect_b32 s25, 0, s25\n" \
    "s_lshl_b32 s26, s25, 11\n" \
    "v_add_u32 v18, s26, v1\n" \
    "s_lshl_b32 s27, s25, 10\n" \
    "s_add_u32 s27, s27, 57344\n" \
    "v_add_u32 v26, s27, v1\n" \
    "s_add_u32 s25, s25, 1\n" \
    "s_cmp_eq_u32 s25, 7\n" \
    "s_cselect_b32 s25, 0, s25\n" \
    "v_lshrrev_b32 v153, 4, v0\n" \
    "v_and_b32 v154, 1, v0\n" \
    "v_cmp_eq_u32 vcc, 1, v154\n" \
    "v_mov_b32 v2, 0x4444\n" \
    "v_mov_b32 v155, 0xeeee\n" \
    "v_cndmask_b32 v2, v2, v155, vcc\n" \
    "v_mov_b32 v5, 0x05040100\n" \
    "v_mov_b32 v155, 0x03020706\n" \
    "v_cndmask_b32 v5, v5, v155, vcc\n" \
    "v_add_u32 v3, 64512, v1\n" \
    "v_and_b32 v155, 14, v0\n" \
    "s_lshl_b32 s24, s14, 11\n" \
    "v_and_b32 v155, 8, v0\n" \
    "v_lshl_add_u32 v7, v155, 6, s24\n" \
    "v_cmp_eq_u32 vcc, 8, v155\n" \
    "v_mov_b32 v6, 0x4444\n" \
    "v_mov_b32 v155, 0xeeee\n" \
    "v_cndmask_b32 v6, v6, v155, vcc\n" \
    "v_and_b32 v155, 7, v0\n" \
    "v_lshl_add_u32 v7, v155, 5, v7\n" \
    "v_and_b32 v155, 32, v0\n" \
    "v_lshl_add_u32 v7, v155, 3, v7\n" \
    "v_and_b32 v155, 16, v0\n" \
    "v_add_u32 v7, v7, v155\n" \
    "s_lshl_b32 s25, s12, 8\n" \
    "v_lshl_add_u32 v4, v153, 6, s25\n" \
    "v_lshl_add_u32 v4, v154, 5, v4\n" \
    "v_and_b32 v155, 2, v0\n" \
    "v_lshl_add_u32 v4, v155, 3, v4\n" \
    "v_and_b32 v155, 12, v0\n" \
    "v_add_u32 v4, v4, v155\n" \
    "v_lshlrev_b32 v8, 7, v152\n" \
    "v_lshl_add_u32 v8, v153, 28, v8\n" \
    "s_lshl_b32 s25, s14, 26\n" \
    "v_or_b32 v19, s25, v8\n" \
    "s_xor_b32 s25, s25, 0x4000000\n" \
    "v_or_b32 v27, s25, v8\n" \
    "s_add_u32 s24, s24, 64512\n" \
    "v_add_u32 v148, s24, v1\n" \
    "v_add_u32 v149, 1024, v148\n" \
    "v_lshl_add_u32 v9, v152, 4, s24\n" \
    "v_lshrrev_b32 v155, 1, v153\n" \
    "v_lshl_add_u32 v9, v155, 8, v9\n" \
    "v_and_b32 v155, 1, v153\n" \
    "v_lshl_add_u32 v9, v155, 3, v9\n" \
    "v_subrev_u32 v11, 57344, v21\n" \
    "v_subrev_u32 v10, 57344, v20\n" \
    "s_waitcnt vmcnt(16)\n" \
    "s_barrier\n" \
    "s_lshl_b32 s24, s14, 10\n" \
    "v_add_u32 v250, s24, v1\n" \
    "s_cmp_lt_u32 s14, 2\n" \
    "s_cselect_b32 s25, 0, 0x40000000\n" \
    "v_add_u32 v3, s25, v250\n" \
    "v_and_b32 v154, 3, v0\n" \
    "s_lshl_b32 s24, s14, 8\n" \
    "v_lshl_add_u32 v251, v154, 6, s24\n" \
    "s_lshl_b32 s24, s14, 12\n" \
    "s_add_u32 s24, s24, 64512\n" \
    "v_add_u32 v252, s24, v1\n" \
    "s_and_b32 s24, s14, 1\n" \
    "s_lshl_b32 s25, s24, 11\n" \
    "s_add_u32 s25, s25, 64512\n" \
    "v_add_u32 v253, s25, v1\n" \
    "s_lshl_b32 s25, s24, 6\n" \
    "v_lshl_add_u32 v7, v152, 2, s25\n" \
    "s_mul_i32 s24, s24, 14336\n" \
    "s_lshl_b32 s25, s12, 5\n" \
    "s_add_u32 s24, s24, s25\n" \
    "v_mul_u32_u24 v254, 896, v152\n" \
    "v_lshl_add_u32 v254, v153, 3, v254\n" \
    "v_add_u32 v254, s24, v254\n" \
    "v_lshlrev_b32 v255, 2, v0\n" \
    "v_lshlrev_b32 v154, 6, v0\n" \
    "s_mov_b32 s26, 24182784\n" \
    "s_mov_b32 s27, 24186880\n" \
    "LFwaito_%=:\n" \
    "s_sleep 8\n" \
    "buffer_load_dword v155, v154, s[4:7], s26 offen sc1\n" \
    "buffer_load_dword v153, v154, s[4:7], s27 offen sc1\n" \
    "s_sub_u32 s17, s17, 1\n" \
    "s_cmp_lt_i32 s17, 0\n" \
    "s_cselect_b64 s[28:29], 0, -1\n" \
    "s_waitcnt vmcnt(0)\n" \
    "v_min_u32 v155, v155, v153\n" \
    "v_cmp_eq_u32 vcc, 0, v155\n" \
    "s_nop 0\n" \
    "s_and_b64 vcc, vcc, s[28:29]\n" \
    "s_cbranch_scc1 LFwaito_%=\n" \
    "LFturn_%=:\n" \
    "ds_read_b128 v[28:31], v12 offset:0\n" \
    "ds_read_b128 v[32:35], v12 offset:14336\n" \
    "s_add_u32 s19, s19, s20\n" \
    "s_cmp_lt_u32 s13, 2\n" \
    "s_cselect_b64 vcc, -1, 0\n" \
    "s_waitcnt lgkmcnt(0)\n" \
    "v_mfma_f32_16x16x32_bf16 v[44:47], v[28:31], a[0:3], 0\n" \
    "ds_read_b128 v[36:39], v12 offset:1024\n" \
    "ds_read_b128 v[40:43], v12 offset:15360\n" \
    "v_mfma_f32_16x16x32_bf16 v[48:51], v[32:35], a[0:3], 0\n" \
    "s_lshr_b32 s24, s13, 3\n" \
    "s_and_b32 s24, s24, 2\n" \
    "v_mfma_f32_16x16x32_bf16 v[52:55], v[28:31], a[8:11], 0\n" \
    "s_lshl_b32 s25, s13, 15\n" \
    "s_xor_b32 s22, s24, s15\n" \
    "v_mfma_f32_16x16x32_bf16 v[56:59], v[32:35], a[8:11], 0\n" \
    "s_xor_b32 s22, s22, 2\n" \
    "s_lshl_b32 s22, s22, 13\n" \
    "v_mfma_f32_16x16x32_bf16 v[60:63], v[28:31], a[16:19], 0\n" \
    "s_add_u32 s22, s22, s25\n" \
    "s_xor_b32 s35, s24, s16\n" \
    "v_mfma_f32_16x16x32_bf16 v[64:67], v[32:35], a[16:19], 0\n" \
    "s_lshl_b32 s35, s35, 13\n" \
    "s_add_u32 s35, s35, s25\n" \
    "v_mfma_f32_16x16x32_bf16 v[68:71], v[28:31], a[24:27], 0\n" \
    "s_add_u32 s35, s35, 2097152\n" \
    "s_xor_b32 s23, s35, 0x4000\n" \
    "v_mfma_f32_16x16x32_bf16 v[72:75], v[32:35], a[24:27], 0\n" \
    "s_cmp_eq_u32 s13, 0\n" \
    "s_cselect_b32 s21, 24203264, s35\n" \
    "s_mul_i32 s30, s13, 28672\n" \
    "s_add_u32 s31, s30, 20971520\n" \
    "s_waitcnt lgkmcnt(0)\n" \
    "v_mfma_f32_16x16x32_bf16 v[44:47], v[36:39], a[4:7], v[44:47]\n" \
    "ds_read_b128 v[28:31], v13 offset:0\n" \
    "ds_read_b128 v[32:35], v13 offset:14336\n" \
    "v_mfma_f32_16x16x32_bf16 v[48:51], v[40:43], a[4:7], v[48:51]\n" \
    "s_lshl_b32 s38, s13, 7\n" \
    "s_add_u32 s38, s38, 23068672\n" \
    "v_mfma_f32_16x16x32_bf16 v[52:55], v[36:39], a[12:15], v[52:55]\n" \
    "buffer_load_dword v248, v7, s[4:7], s38 offen sc1\n" \
    "buffer_load_dwordx4 v[216:219], v250, s[4:7], s31 offen sc1\n" \
    "v_mfma_f32_16x16x32_bf16 v[56:59], v[40:43], a[12:15], v[56:59]\n" \
    "s_add_u32 s31, s31, 4096\n" \
    "buffer_load_dwordx4 v[220:223], v250, s[4:7], s31 offen sc1\n" \
    "v_mfma_f32_16x16x32_bf16 v[60:63], v[36:39], a[20:23], v[60:63]\n" \
    "s_add_u32 s31, s31, 4096\n" \
    "buffer_load_dwordx4 v[224:227], v250, s[4:7], s31 offen sc1\n" \
    "v_mfma_f32_16x16x32_bf16 v[64:67], v[40:43], a[20:23], v[64:67]\n" \
    "s_add_u32 s31, s31, 4096\n" \
    "buffer_load_dwordx4 v[228:231], v3, s[4:7], s31 offen sc1\n" \
    "v_mfma_f32_16x16x32_bf16 v[68:71], v[36:39], a[28:31], v[68:71]\n" \
    "s_add_u32 s31, s31, 2048\n" \
    "buffer_load_dwordx4 v[232:235], v250, s[4:7], s31 offen sc1\n" \
    "v_mfma_f32_16x16x32_bf16 v[72:75], v[40:43], a[28:31], v[72:75]\n" \
    "s_add_u32 s31, s31, 4096\n" \
    "buffer_load_dwordx4 v[236:239], v250, s[4:7], s31 offen sc1\n" \
    "s_add_u32 s31, s31, 4096\n" \
    "buffer_load_dwordx4 v[240:243], v250, s[4:7], s31 offen sc1\n" \
    "s_waitcnt lgkmcnt(0)\n" \
    "v_mfma_f32_16x16x32_bf16 v[44:47], v[28:31], a[32:35], v[44:47]\n" \
    "ds_read_b128 v[36:39], v13 offset:1024\n" \
    "ds_read_b128 v[40:43], v13 offset:15360\n" \
    "v_mfma_f32_16x16x32_bf16 v[48:51], v[32:35], a[32:35], v[48:51]\n" \
    "s_add_u32 s31, s31, 4096\n" \
    "buffer_load_dwordx4 v[244:247], v3, s[4:7], s31 offen sc1\n" \
    "v_mfma_f32_16x16x32_bf16 v[52:55], v[28:31], a[40:43], v[52:55]\n" \
    "s_add_u32 s31, s31, 2048\n" \
    "s_lshr_b32 s24, s13, 3\n" \
    "v_mfma_f32_16x16x32_bf16 v[56:59], v[32:35], a[40:43], v[56:59]\n" \
    "s_and_b32 s24, s24, 2\n" \
    "s_xor_b32 s24, s24, s14\n" \
    "v_mfma_f32_16x16x32_bf16 v[60:63], v[28:31], a[48:51], v[60:63]\n" \
    "s_xor_b32 s24, s24, 1\n" \
    "s_lshl_b32 s22, s24, 13\n" \
    "v_mfma_f32_16x16x32_bf16 v[64:67], v[32:35], a[48:51], v[64:67]\n" \
    "s_add_u32 s22, s22, s25\n" \
    "s_and_b32 s24, s14, 1\n" \
    "v_mfma_f32_16x16x32_bf16 v[68:71], v[28:31], a[56:59], v[68:71]\n" \
    "s_lshl_b32 s24, s24, 21\n" \
    "s_add_u32 s22, s22, s24\n" \
    "v_mfma_f32_16x16x32_bf16 v[72:75], v[32:35], a[56:59], v[72:75]\n" \
    "s_add_u32 s2, s22, 4096\n" \
    "s_lshl_b32 s23, s13, 3\n" \
    "s_add_u32 s23, s23, s14\n" \
    "s_lshl_b32 s23, s23, 11\n" \
    "v_add_u32 v12, s20, v12\n" \
    "s_waitcnt lgkmcnt(0)\n" \
    "v_mfma_f32_16x16x32_bf16 v[44:47], v[36:39], a[36:39], v[44:47]\n" \
    "ds_read_b128 v[28:31], v14 offset:0\n" \
    "ds_read_b128 v[32:35], v14 offset:14336\n" \
    "v_mfma_f32_16x16x32_bf16 v[48:51], v[40:43], a[36:39], v[48:51]\n" \
    "s_add_u32 s23, s23, 18874368\n" \
    "s_lshl_b32 s38, s13, 10\n" \
    "v_mfma_f32_16x16x32_bf16 v[52:55], v[36:39], a[44:47], v[52:55]\n" \
    "s_add_u32 s38, s38, 24117248\n" \
    "s_xor_b32 s24, s35, 2105344\n" \
    "v_mfma_f32_16x16x32_bf16 v[56:59], v[40:43], a[44:47], v[56:59]\n" \
    "s_add_u32 s24, s24, 7168\n" \
    "s_cmp_lt_u32 s13, 60\n" \
    "v_mfma_f32_16x16x32_bf16 v[60:63], v[36:39], a[52:55], v[60:63]\n" \
    "s_cselect_b32 s38, s38, s24\n" \
    "s_cselect_b32 s3, 0, -1\n" \
    "v_mfma_f32_16x16x32_bf16 v[64:67], v[40:43], a[52:55], v[64:67]\n" \
    "s_sub_u32 s25, s13, 1\n" \
    "s_lshr_b32 s24, s25, 3\n" \
    "v_mfma_f32_16x16x32_bf16 v[68:71], v[36:39], a[60:63], v[68:71]\n" \
    "s_and_b32 s24, s24, 2\n" \
    "s_xor_b32 s24, s24, s16\n" \
    "v_mfma_f32_16x16x32_bf16 v[72:75], v[40:43], a[60:63], v[72:75]\n" \
    "s_lshl_b32 s24, s24, 13\n" \
    "s_lshl_b32 s25, s25, 15\n" \
    "s_add_u32 s39, s24, s25\n" \
    "s_add_u32 s39, s39, 2104320\n" \
    "s_waitcnt lgkmcnt(0)\n" \
    "v_mfma_f32_16x16x32_bf16 v[44:47], v[28:31], a[64:67], v[44:47]\n" \
    "ds_read_b128 v[36:39], v14 offset:1024\n" \
    "ds_read_b128 v[40:43], v14 offset:15360\n" \
    "v_mfma_f32_16x16x32_bf16 v[48:51], v[32:35], a[64:67], v[48:51]\n" \
    "s_cmp_eq_u32 s13, 0\n" \
    "s_cselect_b32 s39, 24203264, s39\n" \
    "v_mfma_f32_16x16x32_bf16 v[52:55], v[28:31], a[72:75], v[52:55]\n" \
    "v_mov_b32 v120, 0\n" \
    "v_mov_b32 v121, 0\n" \
    "v_mfma_f32_16x16x32_bf16 v[56:59], v[32:35], a[72:75], v[56:59]\n" \
    "v_mov_b32 v122, 0\n" \
    "v_mov_b32 v123, 0\n" \
    "v_mfma_f32_16x16x32_bf16 v[60:63], v[28:31], a[80:83], v[60:63]\n" \
    "v_mov_b32 v124, 0\n" \
    "v_mov_b32 v125, 0\n" \
    "v_mfma_f32_16x16x32_bf16 v[64:67], v[32:35], a[80:83], v[64:67]\n" \
    "v_mov_b32 v126, 0\n" \
    "v_mov_b32 v127, 0\n" \
    "v_mfma_f32_16x16x32_bf16 v[68:71], v[28:31], a[88:91], v[68:71]\n" \
    "v_mov_b32 v128, 0\n" \
    "v_mov_b32 v129, 0\n" \
    "v_mfma_f32_16x16x32_bf16 v[72:75], v[32:35], a[88:91], v[72:75]\n" \
    "v_mov_b32 v130, 0\n" \
    "v_mov_b32 v131, 0\n" \
    "v_mov_b32 v132, 0\n" \
    "v_add_u32 v13, s20, v13\n" \
    "s_waitcnt lgkmcnt(0)\n" \
    "v_mfma_f32_16x16x32_bf16 v[44:47], v[36:39], a[68:71], v[44:47]\n" \
    "ds_read_b128 v[28:31], v15 offset:0\n" \
    "ds_read_b128 v[32:35], v15 offset:14336\n" \
    "v_mfma_f32_16x16x32_bf16 v[48:51], v[40:43], a[68:71], v[48:51]\n" \
    "v_mov_b32 v133, 0\n" \
    "v_mov_b32 v134, 0\n" \
    "v_mfma_f32_16x16x32_bf16 v[52:55], v[36:39], a[76:79], v[52:55]\n" \
    "v_mov_b32 v135, 0\n" \
    "s_add_u32 s26, s13, 4\n" \
    "v_mfma_f32_16x16x32_bf16 v[56:59], v[40:43], a[76:79], v[56:59]\n" \
    "s_min_u32 s26, s26, 63\n" \
    "s_lshl_b32 s26, s26, 2\n" \
    "v_mfma_f32_16x16x32_bf16 v[60:63], v[36:39], a[84:87], v[60:63]\n" \
    "s_add_u32 s26, s26, s14\n" \
    "s_lshl_b32 s26, s26, 14\n" \
    "v_mfma_f32_16x16x32_bf16 v[64:67], v[40:43], a[84:87], v[64:67]\n" \
    "s_lshr_b32 s27, s26, 8\n" \
    "s_and_b32 s27, s27, 0x2000\n" \
    "v_mfma_f32_16x16x32_bf16 v[68:71], v[36:39], a[92:95], v[68:71]\n" \
    "s_add_u32 s26, s26, s27\n" \
    "s_add_u32 s26, s26, 44040192\n" \
    "v_mfma_f32_16x16x32_bf16 v[72:75], v[40:43], a[92:95], v[72:75]\n" \
    "s_mul_i32 s27, s14, 7168\n" \
    "s_add_u32 m0, s27, s19\n" \
    "s_nop 0\n" \
    "s_waitcnt lgkmcnt(0)\n" \
    "v_mfma_f32_16x16x32_bf16 v[44:47], v[28:31], a[96:99], v[44:47]\n" \
    "ds_read_b128 v[36:39], v15 offset:1024\n" \
    "ds_read_b128 v[40:43], v15 offset:15360\n" \
    "v_mfma_f32_16x16x32_bf16 v[48:51], v[32:35], a[96:99], v[48:51]\n" \
    "v_mfma_f32_16x16x32_bf16 v[52:55], v[28:31], a[104:107], v[52:55]\n" \
    "v_mfma_f32_16x16x32_bf16 v[56:59], v[32:35], a[104:107], v[56:59]\n" \
    "buffer_load_dwordx4 v1, s[4:7], s26 offen offset:0 lds\n" \
    "v_add_u32 v14, s20, v14\n" \
    "v_mfma_f32_16x16x32_bf16 v[60:63], v[28:31], a[112:115], v[60:63]\n" \
    "v_mfma_f32_16x16x32_bf16 v[64:67], v[32:35], a[112:115], v[64:67]\n" \
    "v_mfma_f32_16x16x32_bf16 v[68:71], v[28:31], a[120:123], v[68:71]\n" \
    "v_mfma_f32_16x16x32_bf16 v[72:75], v[32:35], a[120:123], v[72:75]\n" \
    "s_waitcnt lgkmcnt(0)\n" \
    "v_mfma_f32_16x16x32_bf16 v[44:47], v[36:39], a[100:103], v[44:47]\n" \
    "ds_read_b128 v[28:31], v16 offset:0\n" \
    "ds_read_b128 v[32:35], v16 offset:14336\n" \
    "v_mfma_f32_16x16x32_bf16 v[48:51], v[40:43], a[100:103], v[48:51]\n" \
    "v_mfma_f32_16x16x32_bf16 v[52:55], v[36:39], a[108:111], v[52:55]\n" \
    "v_mfma_f32_16x16x32_bf16 v[56:59], v[40:43], a[108:111], v[56:59]\n" \
    "buffer_load_dwordx4 v1, s[4:7], s26 offen offset:1024 lds\n" \
    "v_mfma_f32_16x16x32_bf16 v[60:63], v[36:39], a[116:119], v[60:63]\n" \
    "v_mfma_f32_16x16x32_bf16 v[64:67], v[40:43], a[116:119], v[64:67]\n" \
    "v_mfma_f32_16x16x32_bf16 v[68:71], v[36:39], a[124:127], v[68:71]\n" \
    "v_mfma_f32_16x16x32_bf16 v[72:75], v[40:43], a[124:127], v[72:75]\n" \
    "s_waitcnt lgkmcnt(0)\n" \
    "v_mfma_f32_16x16x32_bf16 v[44:47], v[28:31], a[128:131], v[44:47]\n" \
    "ds_read_b128 v[36:39], v16 offset:1024\n" \
    "ds_read_b128 v[40:43], v16 offset:15360\n" \
    "v_mfma_f32_16x16x32_bf16 v[48:51], v[32:35], a[128:131], v[48:51]\n" \
    "v_mfma_f32_16x16x32_bf16 v[52:55], v[28:31], a[136:139], v[52:55]\n" \
    "v_mfma_f32_16x16x32_bf16 v[56:59], v[32:35], a[136:139], v[56:59]\n" \
    "buffer_load_dwordx4 v1, s[4:7], s26 offen offset:2048 lds\n" \
    "v_add_u32 v15, s20, v15\n" \
    "v_mfma_f32_16x16x32_bf16 v[60:63], v[28:31], a[144:147], v[60:63]\n" \
    "v_mfma_f32_16x16x32_bf16 v[64:67], v[32:35], a[144:147], v[64:67]\n" \
    "v_mfma_f32_16x16x32_bf16 v[68:71], v[28:31], a[152:155], v[68:71]\n" \
    "v_mfma_f32_16x16x32_bf16 v[72:75], v[32:35], a[152:155], v[72:75]\n" \
    "s_waitcnt lgkmcnt(0)\n" \
    "v_mfma_f32_16x16x32_bf16 v[44:47], v[36:39], a[132:135], v[44:47]\n" \
    "ds_read_b128 v[28:31], v17 offset:0\n" \
    "ds_read_b128 v[32:35], v17 offset:14336\n" \
    "v_mfma_f32_16x16x32_bf16 v[48:51], v[40:43], a[132:135], v[48:51]\n" \
    "v_mfma_f32_16x16x32_bf16 v[52:55], v[36:39], a[140:143], v[52:55]\n" \
    "v_mfma_f32_16x16x32_bf16 v[56:59], v[40:43], a[140:143], v[56:59]\n" \
    "buffer_load_dwordx4 v1, s[4:7], s26 offen offset:3072 lds\n" \
    "v_mfma_f32_16x16x32_bf16 v[60:63], v[36:39], a[148:151], v[60:63]\n" \
    "v_mfma_f32_16x16x32_bf16 v[64:67], v[40:43], a[148:151], v[64:67]\n" \
    "v_mfma_f32_16x16x32_bf16 v[68:71], v[36:39], a[156:159], v[68:71]\n" \
    "v_mfma_f32_16x16x32_bf16 v[72:75], v[40:43], a[156:159], v[72:75]\n" \
    "s_waitcnt lgkmcnt(0)\n" \
    "v_mfma_f32_16x16x32_bf16 v[44:47], v[28:31], a[160:163], v[44:47]\n" \
    "ds_read_b128 v[36:39], v17 offset:1024\n" \
    "ds_read_b128 v[40:43], v17 offset:15360\n" \
    "v_mfma_f32_16x16x32_bf16 v[48:51], v[32:35], a[160:163], v[48:51]\n" \
    "v_mfma_f32_16x16x32_bf16 v[52:55], v[28:31], a[168:171], v[52:55]\n" \
    "v_mfma_f32_16x16x32_bf16 v[56:59], v[32:35], a[168:171], v[56:59]\n" \
    "s_add_u32 m0, m0, 4096\n" \
    "s_add_u32 s26, s26, 4096\n" \
    "buffer_load_dwordx4 v1, s[4:7], s26 offen offset:0 lds\n" \
    "v_mfma_f32_16x16x32_bf16 v[60:63], v[28:31], a[176:179], v[60:63]\n" \
    "v_add_u32 v16, s20, v16\n" \
    "v_mfma_f32_16x16x32_bf16 v[64:67], v[32:35], a[176:179], v[64:67]\n" \
    "v_mfma_f32_16x16x32_bf16 v[68:71], v[28:31], a[184:187], v[68:71]\n" \
    "v_mfma_f32_16x16x32_bf16 v[72:75], v[32:35], a[184:187], v[72:75]\n" \
    "s_waitcnt lgkmcnt(0)\n" \
    "v_mfma_f32_16x16x32_bf16 v[44:47], v[36:39], a[164:167], v[44:47]\n" \
    "ds_read_b128 v[28:31], v18 offset:0\n" \
    "ds_read_b128 v[32:35], v18 offset:14336\n" \
    "v_mfma_f32_16x16x32_bf16 v[48:51], v[40:43], a[164:167], v[48:51]\n" \
    "v_mfma_f32_16x16x32_bf16 v[52:55], v[36:39], a[172:175], v[52:55]\n" \
    "v_mfma_f32_16x16x32_bf16 v[56:59], v[40:43], a[172:175], v[56:59]\n" \
    "buffer_load_dwordx4 v1, s[4:7], s26 offen offset:1024 lds\n" \
    "v_mfma_f32_16x16x32_bf16 v[60:63], v[36:39], a[180:183], v[60:63]\n" \
    "v_mfma_f32_16x16x32_bf16 v[64:67], v[40:43], a[180:183], v[64:67]\n" \
    "v_mfma_f32_16x16x32_bf16 v[68:71], v[36:39], a[188:191], v[68:71]\n" \
    "v_mfma_f32_16x16x32_bf16 v[72:75], v[40:43], a[188:191], v[72:75]\n" \
    "s_waitcnt lgkmcnt(0)\n" \
    "v_mfma_f32_16x16x32_bf16 v[44:47], v[28:31], a[192:195], v[44:47]\n" \
    "ds_read_b128 v[36:39], v18 offset:1024\n" \
    "ds_read_b128 v[40:43], v18 offset:15360\n" \
    "v_mfma_f32_16x16x32_bf16 v[48:51], v[32:35], a[192:195], v[48:51]\n" \
    "v_mfma_f32_16x16x32_bf16 v[52:55], v[28:31], a[200:203], v[52:55]\n" \
    "v_mfma_f32_16x16x32_bf16 v[56:59], v[32:35], a[200:203], v[56:59]\n" \
    "buffer_load_dwordx4 v1, s[4:7], s26 offen offset:2048 lds\n" \
    "v_add_u32 v17, s20, v17\n" \
    "v_mfma_f32_16x16x32_bf16 v[60:63], v[28:31], a[208:211], v[60:63]\n" \
    "v_mfma_f32_16x16x32_bf16 v[64:67], v[32:35], a[208:211], v[64:67]\n" \
    "v_mfma_f32_16x16x32_bf16 v[68:71], v[28:31], a[216:219], v[68:71]\n" \
    "v_mfma_f32_16x16x32_bf16 v[72:75], v[32:35], a[216:219], v[72:75]\n" \
    "s_waitcnt lgkmcnt(0)\n" \
    "v_mfma_f32_16x16x32_bf16 v[44:47], v[36:39], a[196:199], v[44:47]\n" \
    "v_add_u32 v18, s20, v18\n" \
    "s_sub_u32 s20, 0, s20\n" \
    "v_mfma_f32_16x16x32_bf16 v[48:51], v[40:43], a[196:199], v[48:51]\n" \
    "v_mfma_f32_16x16x32_bf16 v[52:55], v[36:39], a[204:207], v[52:55]\n" \
    "v_mfma_f32_16x16x32_bf16 v[56:59], v[40:43], a[204:207], v[56:59]\n" \
    "v_mfma_f32_16x16x32_bf16 v[60:63], v[36:39], a[212:215], v[60:63]\n" \
    "v_mfma_f32_16x16x32_bf16 v[64:67], v[40:43], a[212:215], v[64:67]\n" \
    "v_mfma_f32_16x16x32_bf16 v[68:71], v[36:39], a[220:223], v[68:71]\n" \
    "v_mfma_f32_16x16x32_bf16 v[72:75], v[40:43], a[220:223], v[72:75]\n" \
    "s_waitcnt vmcnt(7)\n" \
    "v_mfma_f32_16x16x32_bf16 v[144:147], v[200:203], v[216:219], 0\n" \
    "v_mfma_f32_16x16x32_bf16 v[144:147], v[204:207], v[220:223], v[144:147]\n" \
    "v_mfma_f32_16x16x32_bf16 v[144:147], v[208:211], v[224:227], v[144:147]\n" \
    "v_mfma_f32_16x16x32_bf16 v[144:147], v[212:215], v[228:231], v[144:147]\n" \
    "v_mfma_f32_16x16x32_bf16 v[148:151], v[200:203], v[232:235], 0\n" \
    "v_mfma_f32_16x16x32_bf16 v[148:151], v[204:207], v[236:239], v[148:151]\n" \
    "v_mfma_f32_16x16x32_bf16 v[148:151], v[208:211], v[240:243], v[148:151]\n" \
    "v_mfma_f32_16x16x32_bf16 v[148:151], v[212:215], v[244:247], v[148:151]\n" \
    "v_cvt_pk_bf16_f32 v76, v44, v45\n" \
    "v_cvt_pk_bf16_f32 v77, v46, v47\n" \
    "v_cvt_pk_bf16_f32 v78, v48, v49\n" \
    "v_cvt_pk_bf16_f32 v79, v50, v51\n" \
    "v_cvt_pk_bf16_f32 v80, v52, v53\n" \
    "v_cvt_pk_bf16_f32 v81, v54, v55\n" \
    "v_cvt_pk_bf16_f32 v82, v56, v57\n" \
    "v_cvt_pk_bf16_f32 v83, v58, v59\n" \
    "v_cvt_pk_bf16_f32 v84, v60, v61\n" \
    "v_cvt_pk_bf16_f32 v85, v62, v63\n" \
    "v_cvt_pk_bf16_f32 v86, v64, v65\n" \
    "v_cvt_pk_bf16_f32 v87, v66, v67\n" \
    "v_cvt_pk_bf16_f32 v88, v68, v69\n" \
    "v_cvt_pk_bf16_f32 v89, v70, v71\n" \
    "v_cvt_pk_bf16_f32 v90, v72, v73\n" \
    "v_cvt_pk_bf16_f32 v91, v74, v75\n" \
    "ds_write_b128 v252, v[144:147] offset:0\n" \
    "ds_write_b128 v252, v[148:151] offset:2048\n" \
    "s_waitcnt lgkmcnt(0)\n" \
    "s_cmp_lg_u32 s14, 0\n" \
    "s_cbranch_scc1 LFgo_%=\n" \
    "v_mov_b32 v154, 0\n" \
    "LFpoll_%=:\n" \
    "s_sleep 2\n" \
    "buffer_load_dword v153, v154, s[4:7], s39 offen sc1\n" \
    "s_sub_u32 s17, s17, 1\n" \
    "s_cmp_lt_i32 s17, 0\n" \
    "s_cselect_b64 s[28:29], 0, -1\n" \
    "s_waitcnt vmcnt(0)\n" \
    "v_cmp_eq_u32 vcc, -1, v153\n" \
    "s_nop 0\n" \
    "s_and_b64 vcc, vcc, s[28:29]\n" \
    "s_cbranch_scc1 LFpoll_%=\n" \
    "s_sleep 4\n" \
    "LFgo_%=:\n" \
    "s_barrier\n" \
    "LFmem_%=:\n" \
    "buffer_load_dwordx4 v[92:95], v10, s[4:7], s21 offen sc1\n" \
    "buffer_load_dwordx4 v[96:99], v11, s[4:7], s21 offen sc1\n" \
    "ds_read_b128 v[160:163], v253 offset:0\n" \
    "ds_read_b128 v[164:167], v253 offset:4096\n" \
    "ds_read_b128 v[168:171], v253 offset:8192\n" \
    "ds_read_b128 v[172:175], v253 offset:12288\n" \
    "v_sub_f32 v249, 1.0, v248\n" \
    "v_cvt_pk_bf16_f32 v249, v249, v249\n" \
    "v_and_b32 v249, 0xffff0000, v249\n" \
    "s_waitcnt lgkmcnt(2)\n" \
    "v_add_f32 v160, v160, v164\n" \
    "v_add_f32 v161, v161, v165\n" \
    "v_add_f32 v162, v162, v166\n" \
    "v_add_f32 v163, v163, v167\n" \
    "s_waitcnt lgkmcnt(0)\n" \
    "v_add_f32 v168, v168, v172\n" \
    "v_add_f32 v160, v160, v168\n" \
    "v_add_f32 v169, v169, v173\n" \
    "v_add_f32 v161, v161, v169\n" \
    "v_add_f32 v170, v170, v174\n" \
    "v_add_f32 v162, v162, v170\n" \
    "v_add_f32 v171, v171, v175\n" \
    "v_add_f32 v163, v163, v171\n" \
    "v_cvt_pk_bf16_f32 v160, v160, v161\n" \
    "v_and_b32 v161, 0xffff0000, v160\n" \
    "v_lshlrev_b32 v160, 16, v160\n" \
    "v_cvt_pk_bf16_f32 v162, v162, v163\n" \
    "v_and_b32 v163, 0xffff0000, v162\n" \
    "v_lshlrev_b32 v162, 16, v162\n" \
    "v_mul_f32 v160, v248, v160\n" \
    "v_mul_f32 v161, v248, v161\n" \
    "v_mul_f32 v162, v248, v162\n" \
    "v_mul_f32 v163, v248, v163\n" \
    "v_cvt_pk_bf16_f32 v160, v160, v161\n" \
    "v_and_b32 v141, 0xffff0000, v160\n" \
    "v_lshlrev_b32 v140, 16, v160\n" \
    "v_cvt_pk_bf16_f32 v162, v162, v163\n" \
    "v_and_b32 v143, 0xffff0000, v162\n" \
    "v_lshlrev_b32 v142, 16, v162\n" \
    "s_sub_u32 s17, s17, 1\n" \
    "s_cmp_lt_i32 s17, 0\n" \
    "s_cselect_b64 s[28:29], 0, -1\n" \
    "s_waitcnt vmcnt(0)\n" \
    "ds_write_b128 v20, v[92:95]\n" \
    "ds_write_b128 v21, v[96:99]\n" \
    "v_max3_u32 v152, v92, v93, v94\n" \
    "v_max3_u32 v152, v152, v95, v96\n" \
    "v_max3_u32 v152, v152, v97, v98\n" \
    "v_max_u32 v152, v152, v99\n" \
    "v_cmp_eq_u32 vcc, -1, v152\n" \
    "s_nop 0\n" \
    "s_and_b64 vcc, vcc, s[28:29]\n" \
    "s_cbranch_scc1 LFmem_%=\n" \
    "s_add_u32 s26, s22, 4096\n" \
    "s_waitcnt lgkmcnt(0)\n" \
    "s_barrier\n" \
    "ds_read_b128 v[100:103], v22\n" \
    "v_smfmac_f32_16x16x64_bf16 v[120:123], v[92:95], a[0:7], v2\n" \
    "ds_read_b128 v[104:107], v23\n" \
    "v_smfmac_f32_16x16x64_bf16 v[124:127], v[92:95], a[8:15], v2\n" \
    "ds_read_b128 v[108:111], v24\n" \
    "v_smfmac_f32_16x16x64_bf16 v[128:131], v[92:95], a[16:23], v2\n" \
    "ds_read_b128 v[112:115], v25\n" \
    "v_smfmac_f32_16x16x64_bf16 v[132:135], v[92:95], a[24:31], v2\n" \
    "ds_read_b128 v[116:119], v26\n" \
    "v_smfmac_f32_16x16x64_bf16 v[120:123], v[96:99], a[32:39], v2\n" \
    "v_smfmac_f32_16x16x64_bf16 v[124:127], v[96:99], a[40:47], v2\n" \
    "buffer_load_dword v153, v251, s[4:7], s38 offen sc1\n" \
    "v_smfmac_f32_16x16x64_bf16 v[128:131], v[96:99], a[48:55], v2\n" \
    "v_smfmac_f32_16x16x64_bf16 v[132:135], v[96:99], a[56:63], v2\n" \
    "s_waitcnt lgkmcnt(4)\n" \
    "v_smfmac_f32_16x16x64_bf16 v[120:123], v[100:103], a[64:71], v2\n" \
    "v_smfmac_f32_16x16x64_bf16 v[124:127], v[100:103], a[72:79], v2\n" \
    "v_smfmac_f32_16x16x64_bf16 v[128:131], v[100:103], a[80:87], v2\n" \
    "v_smfmac_f32_16x16x64_bf16 v[132:135], v[100:103], a[88:95], v2\n" \
    "s_waitcnt lgkmcnt(3)\n" \
    "v_smfmac_f32_16x16x64_bf16 v[120:123], v[104:107], a[96:103], v2\n" \
    "v_smfmac_f32_16x16x64_bf16 v[124:127], v[104:107], a[104:111], v2\n" \
    "v_smfmac_f32_16x16x64_bf16 v[128:131], v[104:107], a[112:119], v2\n" \
    "v_smfmac_f32_16x16x64_bf16 v[132:135], v[104:107], a[120:127], v2\n" \
    "s_waitcnt lgkmcnt(2)\n" \
    "v_smfmac_f32_16x16x64_bf16 v[120:123], v[108:111], a[128:135], v2\n" \
    "v_smfmac_f32_16x16x64_bf16 v[124:127], v[108:111], a[136:143], v2\n" \
    "v_smfmac_f32_16x16x64_bf16 v[128:131], v[108:111], a[144:151], v2\n" \
    "v_smfmac_f32_16x16x64_bf16 v[132:135], v[108:111], a[152:159], v2\n" \
    "s_waitcnt lgkmcnt(0)\n" \
    "v_smfmac_f32_16x16x64_bf16 v[120:123], v[112:115], a[160:167], v2\n" \
    "v_smfmac_f32_16x16x64_bf16 v[124:127], v[112:115], a[168:175], v2\n" \
    "v_smfmac_f32_16x16x64_bf16 v[128:131], v[112:115], a[176:183], v2\n" \
    "v_smfmac_f32_16x16x64_bf16 v[132:135], v[112:115], a[184:191], v2\n" \
    "v_smfmac_f32_16x16x64_bf16 v[120:123], v[116:119], a[192:199], v2\n" \
    "v_smfmac_f32_16x16x64_bf16 v[124:127], v[116:119], a[200:207], v2\n" \
    "v_smfmac_f32_16x16x64_bf16 v[128:131], v[116:119], a[208:215], v2\n" \
    "v_smfmac_f32_16x16x64_bf16 v[132:135], v[116:119], a[216:223], v2\n" \
    "s_nop 4\n" \
    "v_add_f32 v120, v120, v121\n" \
    "v_add_f32 v122, v122, v123\n" \
    "v_cvt_pk_bf16_f32 v136, v120, v122\n" \
    "v_add_f32 v124, v124, v125\n" \
    "v_add_f32 v126, v126, v127\n" \
    "v_cvt_pk_bf16_f32 v137, v124, v126\n" \
    "v_add_f32 v128, v128, v129\n" \
    "v_add_f32 v130, v130, v131\n" \
    "v_cvt_pk_bf16_f32 v138, v128, v130\n" \
    "v_add_f32 v132, v132, v133\n" \
    "v_add_f32 v134, v134, v135\n" \
    "v_cvt_pk_bf16_f32 v139, v132, v134\n" \
    "s_branch LFflag1_%=\n" \
    "LFflag_%=:\n" \
    "s_sleep 1\n" \
    "buffer_load_dword v153, v251, s[4:7], s38 offen sc1\n" \
    "LFflag1_%=:\n" \
    "s_sub_u32 s17, s17, 1\n" \
    "s_cmp_lt_i32 s17, 0\n" \
    "s_cselect_b64 s[28:29], 0, -1\n" \
    "s_waitcnt vmcnt(0)\n" \
    "v_cmp_eq_u32 vcc, s3, v153\n" \
    "s_nop 0\n" \
    "s_and_b64 vcc, vcc, s[28:29]\n" \
    "s_cbranch_scc1 LFflag_%=\n" \
    "s_cmp_lt_u32 s13, 60\n" \
    "s_cbranch_scc1 LFnow_%=\n" \
    "v_readfirstlane_b32 s24, v153\n" \
    "s_nop 0\n" \
    "s_add_u32 s24, s24, 300\n" \
    "LFsoon_%=:\n" \
    "s_memrealtime s[34:35]\n" \
    "s_sub_u32 s17, s17, 1\n" \
    "s_waitcnt lgkmcnt(0)\n" \
    "s_mul_i32 s34, s34, 24\n" \
    "s_sub_u32 s34, s24, s34\n" \
    "s_min_i32 s34, s34, s17\n" \
    "s_cmp_gt_i32 s34, 0\n" \
    "s_cbranch_scc1 LFsoon_%=\n" \
    "LFnow_%=:\n" \
    "LFrows_%=:\n" \
    "buffer_load_dwordx4 v[160:163], v1, s[4:7], s22 offen offset:0 sc1\n" \
    "buffer_load_dwordx4 v[164:167], v1, s[4:7], s22 offen offset:1024 sc1\n" \
    "buffer_load_dwordx4 v[168:171], v1, s[4:7], s22 offen offset:2048 sc1\n" \
    "buffer_load_dwordx4 v[172:175], v1, s[4:7], s22 offen offset:3072 sc1\n" \
    "buffer_load_dwordx4 v[176:179], v1, s[4:7], s2 offen offset:0 sc1\n" \
    "buffer_load_dwordx4 v[180:183], v1, s[4:7], s2 offen offset:1024 sc1\n" \
    "buffer_load_dwordx4 v[184:187], v1, s[4:7], s2 offen offset:2048 sc1\n" \
    "buffer_load_dwordx4 v[188:191], v1, s[4:7], s2 offen offset:3072 sc1\n" \
    "buffer_load_dword v192, v255, s[4:7], s23 offen offset:0 sc1\n" \
    "buffer_load_dword v193, v255, s[4:7], s23 offen offset:512 sc1\n" \
    "buffer_load_dword v194, v255, s[4:7], s23 offen offset:1024 sc1\n" \
    "buffer_load_dword v195, v255, s[4:7], s23 offen offset:1536 sc1\n" \
    "buffer_load_dword v196, v255, s[4:7], s23 offen offset:256 sc1\n" \
    "buffer_load_dword v197, v255, s[4:7], s23 offen offset:768 sc1\n" \
    "buffer_load_dword v198, v255, s[4:7], s23 offen offset:1280 sc1\n" \
    "buffer_load_dword v199, v255, s[4:7], s23 offen offset:1792 sc1\n" \
    "s_sub_u32 s17, s17, 1\n" \
    "s_cmp_lt_i32 s17, 0\n" \
    "s_cselect_b64 s[28:29], 0, -1\n" \
    "v_mov_b32 v152, 0\n" \
    "s_waitcnt vmcnt(15)\n" \
    "v_mfma_f32_16x16x32_bf16 v[144:147], v[76:79], v[160:163], 0\n" \
    "v_max3_u32 v152, v152, v160, v161\n" \
    "v_max3_u32 v152, v152, v162, v163\n" \
    "s_waitcnt vmcnt(14)\n" \
    "v_mfma_f32_16x16x32_bf16 v[148:151], v[76:79], v[164:167], 0\n" \
    "v_max3_u32 v152, v152, v164, v165\n" \
    "v_max3_u32 v152, v152, v166, v167\n" \
    "s_waitcnt vmcnt(13)\n" \
    "v_mfma_f32_16x16x32_bf16 v[144:147], v[80:83], v[168:171], v[144:147]\n" \
    "v_max3_u32 v152, v152, v168, v169\n" \
    "v_max3_u32 v152, v152, v170, v171\n" \
    "s_waitcnt vmcnt(12)\n" \
    "v_mfma_f32_16x16x32_bf16 v[148:151], v[80:83], v[172:175], v[148:151]\n" \
    "v_max3_u32 v152, v152, v172, v173\n" \
    "v_max3_u32 v152, v152, v174, v175\n" \
    "s_waitcnt vmcnt(11)\n" \
    "v_mfma_f32_16x16x32_bf16 v[144:147], v[84:87], v[176:179], v[144:147]\n" \
    "v_max3_u32 v152, v152, v176, v177\n" \
    "v_max3_u32 v152, v152, v178, v179\n" \
    "s_waitcnt vmcnt(10)\n" \
    "v_mfma_f32_16x16x32_bf16 v[148:151], v[84:87], v[180:183], v[148:151]\n" \
    "v_max3_u32 v152, v152, v180, v181\n" \
    "v_max3_u32 v152, v152, v182, v183\n" \
    "s_waitcnt vmcnt(9)\n" \
    "v_mfma_f32_16x16x32_bf16 v[144:147], v[88:91], v[184:187], v[144:147]\n" \
    "v_max3_u32 v152, v152, v184, v185\n" \
    "v_max3_u32 v152, v152, v186, v187\n" \
    "s_waitcnt vmcnt(8)\n" \
    "v_mfma_f32_16x16x32_bf16 v[148:151], v[88:91], v[188:191], v[148:151]\n" \
    "v_max3_u32 v152, v152, v188, v189\n" \
    "v_max3_u32 v152, v152, v190, v191\n" \
    "s_waitcnt vmcnt(4)\n" \
    "v_mfma_f32_16x16x32_bf16 v[144:147], v[136:139], v[192:195], v[144:147]\n" \
    "v_max3_u32 v152, v152, v192, v193\n" \
    "v_max3_u32 v152, v152, v194, v195\n" \
    "s_waitcnt vmcnt(0)\n" \
    "v_mfma_f32_16x16x32_bf16 v[148:151], v[136:139], v[196:199], v[148:151]\n" \
    "v_max3_u32 v152, v152, v196, v197\n" \
    "v_max3_u32 v152, v152, v198, v199\n" \
    "v_cmp_eq_u32 vcc, -1, v152\n" \
    "s_nop 0\n" \
    "s_and_b64 vcc, vcc, s[28:29]\n" \
    "s_cbranch_scc1 LFrows_%=\n" \
    "ds_write_b128 v252, v[144:147] offset:1024\n" \
    "s_nop 0\n" \
    "ds_write_b128 v252, v[148:151] offset:3072\n" \
    "s_cmp_gt_u32 s14, 1\n" \
    "s_waitcnt lgkmcnt(0)\n" \
    "s_barrier\n" \
    "s_cbranch_scc1 LFsync_%=\n" \
    "ds_read_b128 v[160:163], v253 offset:1024\n" \
    "ds_read_b128 v[164:167], v253 offset:5120\n" \
    "ds_read_b128 v[168:171], v253 offset:9216\n" \
    "ds_read_b128 v[172:175], v253 offset:13312\n" \
    "s_waitcnt lgkmcnt(2)\n" \
    "v_add_f32 v160, v160, v164\n" \
    "v_add_f32 v161, v161, v165\n" \
    "v_add_f32 v162, v162, v166\n" \
    "v_add_f32 v163, v163, v167\n" \
    "s_waitcnt lgkmcnt(0)\n" \
    "v_add_f32 v168, v168, v172\n" \
    "v_add_f32 v160, v160, v168\n" \
    "v_add_f32 v169, v169, v173\n" \
    "v_add_f32 v161, v161, v169\n" \
    "v_add_f32 v170, v170, v174\n" \
    "v_add_f32 v162, v162, v170\n" \
    "v_add_f32 v171, v171, v175\n" \
    "v_add_f32 v163, v163, v171\n" \
    "v_cvt_pk_bf16_f32 v160, v160, v161\n" \
    "v_and_b32 v161, 0xffff0000, v160\n" \
    "v_lshlrev_b32 v160, 16, v160\n" \
    "v_cvt_pk_bf16_f32 v162, v162, v163\n" \
    "v_and_b32 v163, 0xffff0000, v162\n" \
    "v_lshlrev_b32 v162, 16, v162\n" \
    "v_mul_f32 v160, v249, v160\n" \
    "v_mul_f32 v161, v249, v161\n" \
    "v_mul_f32 v162, v249, v162\n" \
    "v_mul_f32 v163, v249, v163\n" \
    "v_cvt_pk_bf16_f32 v160, v160, v161\n" \
    "v_and_b32 v161, 0xffff0000, v160\n" \
    "v_lshlrev_b32 v160, 16, v160\n" \
    "v_cvt_pk_bf16_f32 v162, v162, v163\n" \
    "v_and_b32 v163, 0xffff0000, v162\n" \
    "v_lshlrev_b32 v162, 16, v162\n" \
    "v_add_f32 v160, v160, v140\n" \
    "v_add_f32 v161, v161, v141\n" \
    "v_add_f32 v162, v162, v142\n" \
    "v_add_f32 v163, v163, v143\n" \
    "v_cvt_pk_bf16_f32 v160, v160, v161\n" \
    "v_cvt_pk_bf16_f32 v161, v162, v163\n" \
    "buffer_store_dwordx2 v[160:161], v254, s[8:11], s30 offen\n" \
    "LFsync_%=:\n" \
    "s_barrier\n" \
    "s_add_u32 s13, s13, 4\n" \
    "s_cmp_lt_u32 s13, 64\n" \
    "s_cbranch_scc1 LFturn_%=\n" \
    "s_endpgm\n"
DEV void role_s(const Cx& k, const Args& a, int h, int e) {
    asm volatile(S_ASM ::"s"(a.ws), "s"(a.x), "s"(h), "s"(e), "s"(k.w), "i"(0), "i"(1), "i"(WS_END), TI(T_DS), TI(T_DEL + T_D2), TI(T_DRIFT), "i"(T_LATE), TI(T_PAD), "i"(T_THR), TI(T_K2), TI(T_RD), TI(T_RES - T_DEL - T_D2)
                 : "memory", "scc", "vcc", "s4", "s5", "s6", "s7", "s8", "s9", C8("s", 1), "s80", "s81", "s82", "s83", "s84", "s85", "s86", "s66", "s67", "s99", "v255", "a255");
    __builtin_unreachable();
}

DEV void role_o(const Cx& k, const Args& a, int n, int e) {
    asm volatile(O_ASM ::"s"(a.ws), "s"(a.x), "s"(n), "s"(e), "s"(k.w), "i"(0), "i"(1), "i"(WS_END), TI(T_DEL + T_D2), TI(T_DEL), TI(T_MO), TI(T_EX)
                 : "memory", "scc", "vcc", "s4", "s5", "s6", "s7", "s8", "s9", C8("s", 1), "s80", "s81", "s82", "s83", "s84", "s85", "s86", "s66", "s67", "s99", "v255", "a255");
    __builtin_unreachable();
}

DEV float phi(float t) { t = rbf(t); return rbf(rbf(t > 0.f ? t + 1.f : __expf(t)) + 1e-6f); }
DEV void role_a(Cx& k, const Args& a, int h, int tg) {
    const int l = k.l, w = k.w, c = l & 15, g = l >> 4, tid = threadIdx.x;
    const u32 WL = k.sb, KV = WL + 6 * 14336;
    const int tok0 = tg * 256 + w * 64;
    {
        v2u v[42];
        const int cg = tid & 7, r0 = tid >> 3;
#pragma unroll
        for (int i = 0; i < 42; ++i) { const int r = r0 + 32 * (i / 3), part = i % 3; v[i] = *(const v2u*)(a.wqkv + r * 1344 + part * 448 + h * 28 + min(cg, 6) * 4); }
        u32 gv[14];
#pragma unroll
        for (int i = 0; i < 14; ++i) gv[i] = a.gw[r0 + 32 * i];
        __builtin_amdgcn_sched_barrier(0);
        const int pos = 16 * ((r0 >> 2) & 1) + 4 * (r0 >> 3) + (r0 & 3);
#pragma unroll
        for (int i = 0; i < 42; ++i) {
            const int part = i % 3;
            if (cg == 7) v[i] = (v2u){part == 0 ? gv[i / 3] : 0u, 0u};
            lds_w<v2u>(WL + (part * 2 + (cg >> 2)) * 14336 + (i / 3) * 1024 + pos * 32 + (cg & 3) * 8, v[i]);
        }
    }
    v4u xf[14][4];
#pragma unroll
    for (int kap = 0; kap < 14; ++kap)
#pragma unroll
        for (int t = 0; t < 4; ++t) xf[kap][t] = ld16(k.ws, xfrag(((tok0 >> 4) + t) * 2 + kap / 7) + (kap % 7) * 1024 + l * 16);
    bar();
    v4f acc[4][6];
#pragma unroll
    for (int t = 0; t < 4; ++t)
#pragma unroll
        for (int q = 0; q < 6; ++q) acc[t][q] = (v4f){0, 0, 0, 0};
#pragma unroll
    for (int kap = 0; kap < 14; ++kap) {
#pragma unroll
        for (int q = 0; q < 6; ++q) {
            const v2u u0 = __builtin_bit_cast(v2u, __builtin_amdgcn_ds_read_tr16_b64_v4i16((LAS s4v*)(uintptr_t)(WL + q * 14336 + kap * 1024 + l * 8)));
            const v2u u1 = __builtin_bit_cast(v2u, __builtin_amdgcn_ds_read_tr16_b64_v4i16((LAS s4v*)(uintptr_t)(WL + q * 14336 + kap * 1024 + 512 + l * 8)));
            const v4u wf = {u0.x, u0.y, u1.x, u1.y};
#pragma unroll
            for (int t = 0; t < 4; ++t) acc[t][q] = q < 2 ? mfma(wf, xf[kap][t], acc[t][q]) : mfma(xf[kap][t], wf, acc[t][q]);
        }
    }
    v4u PQ[4];
#pragma unroll
    for (int t = 0; t < 4; ++t) {
        if (h == 0 && g == 3) { const float z = rbf(acc[t][1][0]); st4c(k.ws, GATE + (tok0 + t * 16 + c) * 4, __float_as_uint(rbf(1.f / (1.f + __expf(-z))))); }
        PQ[t] = (v4u){pk(phi(acc[t][0][0]), phi(acc[t][0][1])), pk(phi(acc[t][0][2]), phi(acc[t][0][3])), pk(phi(acc[t][1][0]), phi(acc[t][1][1])), pk(phi(acc[t][1][2]), phi(acc[t][1][3]))};
        if (g == 3) PQ[t].z = PQ[t].w = 0;
    }
    v4f kv[2][2];
    kv[0][0] = kv[0][1] = kv[1][0] = kv[1][1] = (v4f){0, 0, 0, 0};
#pragma unroll
    for (int tp = 0; tp < 2; ++tp) {
        v4u PK[2], VV[2];
#pragma unroll
        for (int d = 0; d < 2; ++d) {
            const v4f k0 = acc[2 * tp][2 + d], k1 = acc[2 * tp + 1][2 + d], v0 = acc[2 * tp][4 + d], v1 = acc[2 * tp + 1][4 + d];
            PK[d] = (v4u){pk(phi(k0[0]), phi(k0[1])), pk(phi(k0[2]), phi(k0[3])), pk(phi(k1[0]), phi(k1[1])), pk(phi(k1[2]), phi(k1[3]))};
            VV[d] = (v4u){pk(v0[0], v0[1]), pk(v0[2], v0[3]), pk(v1[0], v1[1]), pk(v1[2], v1[3])};
        }
        if (c >= 12) PK[1] = (v4u){0, 0, 0, 0};
        if (c == 12) VV[1] = (v4u){0x3F803F80u, 0x3F803F80u, 0x3F803F80u, 0x3F803F80u};
#pragma unroll
        for (int d = 0; d < 2; ++d)
#pragma unroll
            for (int ee = 0; ee < 2; ++ee) kv[d][ee] = mfma(PK[d], VV[ee], kv[d][ee]);
    }
#pragma unroll
    for (int i = 0; i < 4; ++i) lds_w<v4f>(KV + (w * 4 + i) * 1024 + l * 16, kv[i >> 1][i & 1]);
    bar();
    {
        const v4f s = (lds_r<v4f>(KV + tid * 16) + lds_r<v4f>(KV + 4096 + tid * 16)) + (lds_r<v4f>(KV + 8192 + tid * 16) + lds_r<v4f>(KV + 12288 + tid * 16));
        st16c(k.ws, KVP + (h * 8 + tg) * 4096 + tid * 16, __builtin_bit_cast(v4u, s));
    }
    drain();
    bar();
    if (tid == 0) st4c(k.ws, KF + (h * 8 + tg) * 64, 1u);
    if (w == 0) wait_words(k, KF + (h * 8 + (l & 7)) * 64, 0u, true);
    bar();
    {
        v4f s = {0, 0, 0, 0};
#pragma unroll
        for (int i = 0; i < 8; ++i) s += __builtin_bit_cast(v4f, ld16c(k.ws, KVP + (h * 8 + i) * 4096 + tid * 16));
        lds_w<v2u>(KV + (w & 1) * 1024 + l * 16 + (w >> 1) * 8, (v2u){pk(s[0], s[1]), pk(s[2], s[3])});
    }
    bar();
    const v4u KT0 = lds_r<v4u>(KV + l * 16), KT1 = lds_r<v4u>(KV + 1024 + l * 16);
#pragma unroll
    for (int t = 0; t < 4; ++t) {
        const v4f n0 = mfma(KT0, PQ[t], (v4f){0, 0, 0, 0}), n1 = mfma(KT1, PQ[t], (v4f){0, 0, 0, 0});
        const float den = rbf(rbf(__shfl(n1[0], 48 + c)) + 1e-6f), r = 1.f / den;
        const int tok = tok0 + t * 16, f0 = 28 * h + 4 * g, f1 = f0 + 16;
        const u32 o = OB + (tok >> 4) * 14336 + c * 16;
        st8c(k.ws, o + (f0 >> 3) * 256 + (f0 & 4) * 2, (v2u){pk(rbf(n0[0]) * r, rbf(n0[1]) * r), pk(rbf(n0[2]) * r, rbf(n0[3]) * r)});
        if (g < 3) st8c(k.ws, o + (f1 >> 3) * 256 + (f1 & 4) * 2, (v2u){pk(rbf(n1[0]) * r, rbf(n1[1]) * r), pk(rbf(n1[2]) * r, rbf(n1[3]) * r)});
    }
    drain();
    bar();
    if (tid == 0) st4c(k.ws, OF + (tg * 16 + h) * 64, 1u);
}

DEV void role_f(const Cx& k, const Args& a, int n, int e) {
    const int l = k.l, w = k.w, c = l & 15, g = l >> 4;
    u32 lo[16], hi[16];
#pragma unroll
    for (int i = 0; i < 16; ++i) {
        const u16* p = a.wo + (32 * min(w + 4 * (i >> 2), 13) + 8 * g + 2 * (i & 3)) * 448 + 16 * n + c;
        lo[i] = p[0]; hi[i] = p[448];
    }
    __builtin_amdgcn_sched_barrier(0);
#pragma unroll
    for (int i = 0; i < 4; ++i)
        lds_w<v4u>(k.sb + w * 4096 + i * 1024 + l * 16, (v4u){lo[4 * i] | hi[4 * i] << 16, lo[4 * i + 1] | hi[4 * i + 1] << 16, lo[4 * i + 2] | hi[4 * i + 2] << 16, lo[4 * i + 3] | hi[4 * i + 3] << 16});
    asm volatile(F_ASM ::"s"(a.ws), "s"(a.out), "s"(n), "s"(e), "s"(k.w), "i"(0), "i"(1), "i"(WS_END)
                 : "memory", "scc", "vcc", "s4", "s5", "s6", "s7", "s8", "s9", C8("s", 1), "s80", "v255", "a255");
    __builtin_unreachable();
}

__global__ void __launch_bounds__(256, 1) k_main(Args a) {
    extern __shared__ __attribute__((aligned(16))) char smem[];
    Cx k;
    k.ws = __builtin_amdgcn_make_buffer_rsrc(a.ws, 0, WS_END, 0x00020000);
    k.sb = (u32)(uintptr_t)(LAS char*)smem;
    k.l = threadIdx.x & 63; k.w = __builtin_amdgcn_readfirstlane(threadIdx.x >> 6);
    k.budget = 300000;
    const v4u lt = ld16(k.ws, LAT + k.l * 16);
    const u32 rot = lane<0>(__builtin_amdgcn_raw_buffer_load_b32(k.ws, LAT + 1024, 0, 0)) & 7u;
    u32 key = min(min(lt.x, lt.y), min(lt.z, lt.w));
    key = min(key, (u32)__builtin_amdgcn_update_dpp(0, key, X1, 0xf, 0xf, true)); key = min(key, (u32)__builtin_amdgcn_update_dpp(0, key, X2, 0xf, 0xf, true));
    key = min(key, (u32)__builtin_amdgcn_update_dpp(0, key, HM, 0xf, 0xf, true)); key = min(key, (u32)__builtin_amdgcn_update_dpp(0, key, RM, 0xf, 0xf, true));
    const u32 cls = min(min(lane<0>(key), lane<16>(key)), min(lane<32>(key), lane<48>(key)));
    k.c0 = 0; k.c1 = 1;
    const int b = blockIdx.x, x = ((b + rot) & 7) ^ (4 * (cls & 1) + ((cls >> 1) & 1)), j = b >> 3;
    if (x >= 4) {
        const int id = (x - 4) * 32 + j;
        role_a(k, a, id & 15, id >> 4);
        if (j < 28) role_f(k, a, j, x - 4);
    } else if (x & 1) role_s(k, a, j & 15, (x >> 1) + 2 * (j >> 4));
    else if (j < 28) role_o(k, a, j, x >> 1);
}

extern "C" void kernel_launch(void* const* d_in, const int* in_sizes, int n_in, void* d_out, int out_size, void* d_ws, size_t ws_size, hipStream_t stream) {
    Args a;
    a.x = (const u16*)d_in[0]; a.wqkv = (const u16*)d_in[1]; a.wo = (const u16*)d_in[2]; a.mem0 = (const u16*)d_in[4];
    a.Wq = (const u16*)d_in[5]; a.Wk = (const u16*)d_in[6]; a.Wv = (const u16*)d_in[7]; a.Wo = (const u16*)d_in[8]; a.gw = (const u16*)d_in[10];
    a.out = (u16*)d_out;
    a.ws = (char*)(((uintptr_t)d_ws + T_WSOFF + 2 * MB - 1) & ~(uintptr_t)(2 * MB - 1));
    constexpr int L1 = 57344 + 6 * 16384 + 256, L2 = 6 * 14336 + 16384;
    (void)hipFuncSetAttribute((const void*)k_main, hipFuncAttributeMaxDynamicSharedMemorySize, L2);
    (void)hipFuncSetAttribute((const void*)k_prep, hipFuncAttributeMaxDynamicSharedMemorySize, L1);
    k_prep<<<256, 512, L1, stream>>>(a);
    k_main<<<256, 256, L2, stream>>>(a);
}
```
